```python
import torch

def _softmax(x, axis=-1):
    return torch.softmax(x, dim=axis)
def _gelu(x):
    return torch.nn.functional.gelu(x, approximate="tanh")
def _silu(x):
    return torch.nn.functional.silu(x)
def _sigmoid(x):
    return torch.sigmoid(x)
def _layernorm(x, axis=-1, eps=1e-5):
    m = x.mean(dim=axis, keepdim=True)
    v = x.var(dim=axis, keepdim=True, unbiased=False)
    return (x - m) / torch.sqrt(v + eps)
def _scatter_add(tgt, idx, src, axis=0):
    return tgt.clone().index_add_(axis, idx, src)
def _segment_sum(src, seg, n):
    out = torch.zeros((n,) + src.shape[1:], dtype=src.dtype)
    return out.index_add_(0, seg, src)
def _pad(x, pw):
    flat = tuple(v for pair in reversed(pw) for v in pair)
    return torch.nn.functional.pad(x, flat)

kernel_name = 'sequential_preLN__mha_block_cached__glu_gated_bilinear__decode'

def setup_inputs():

    _w = (lambda a: (lambda t: t.to(torch.bfloat16) if t.is_floating_point() else t)(torch.as_tensor(a)))
    def _A_setup():
        B, Sq, D = 2, 1, 640
        H, Sk, Dh = 32, 4096, 128
        return {
            "x": _w((lambda *s: torch.randn(*s, dtype=torch.bfloat16) * 0.05)(B, Sq, D)),
            "k_cache": _w((lambda *s: torch.randn(*s, dtype=torch.bfloat16) * 1.6)(B, H, Sk, Dh)),
            "v_cache": _w((lambda *s: torch.randn(*s, dtype=torch.bfloat16) * 0.8)(B, H, Sk, Dh)),
            "wq": _w((lambda *s: torch.randn(*s, dtype=torch.bfloat16) * 1.6)(D, H * Dh) * (D ** -0.5)),
            "wo": _w((lambda *s: torch.randn(*s, dtype=torch.bfloat16) * 0.8)(H * Dh, D) * ((H * Dh) ** -0.5)),
        }
    _A_in = _A_setup()

    def _B_setup():
        B, S, D, F = 2, 1, 640, 1728
        return {
            "x": _w((lambda *s: torch.randn(*s, dtype=torch.bfloat16) * 0.05)(B, S, D)),
            "w_gate": _w((lambda *s: torch.randn(*s, dtype=torch.bfloat16) * 0.4)(D, F) * (D ** -0.5)),
            "w_up": _w((lambda *s: torch.randn(*s, dtype=torch.bfloat16) * 0.4)(D, F) * (D ** -0.5)),
            "w_down": _w((lambda *s: torch.randn(*s, dtype=torch.bfloat16) * 0.4)(F, D) * (F ** -0.5)),
        }
    _B_in = _B_setup()

    return {
        "A_x": _A_in["x"],
        "A_k_cache": _A_in["k_cache"],
        "A_v_cache": _A_in["v_cache"],
        "A_wq": _A_in["wq"],
        "A_wo": _A_in["wo"],
        "B_x": _B_in["x"],
        "B_w_gate": _B_in["w_gate"],
        "B_w_up": _B_in["w_up"],
        "B_w_down": _B_in["w_down"],
    }

def reference(A_x, A_k_cache, A_v_cache, A_wq, A_wo, B_x, B_w_gate, B_w_up, B_w_down):
    def _A_ref(x, k_cache, v_cache, wq, wo):
        B, Sq, D = x.shape
        H, Dh = 32, 128
        q = (lambda x, *d: x.permute(*d))((x @ wq).reshape(B, Sq, H, Dh), 0, 2, 1, 3)
        scores = torch.einsum("bhqd,bhkd->bhqk", q, k_cache) * (Dh ** -0.5)
        p = _softmax(scores, axis=-1)
        o = torch.einsum("bhqk,bhkd->bhqd", p, v_cache)
        return (lambda x, *d: x.permute(*d))(o, 0, 2, 1, 3).reshape(B, Sq, H * Dh) @ wo

    def _B_ref(x, w_gate, w_up, w_down):
        g = torch.einsum("bsd,df->bsf", x, w_gate)
        u = torch.einsum("bsd,df->bsf", x, w_up)
        h = g * u
        return torch.einsum("bsf,fd->bsd", h, w_down)

    x = A_x
    h = x + _A_ref(_layernorm(x, axis=-1), A_k_cache, A_v_cache, A_wq, A_wo)
    h2 = h + _B_ref(_layernorm(h, axis=-1), B_w_gate, B_w_up, B_w_down)
    return h2
```

```cpp
#include <hip/hip_runtime.h>
#include <stdint.h>

#define MIMIC 1

typedef unsigned u32x4 __attribute__((ext_vector_type(4)));
typedef unsigned u32x2 __attribute__((ext_vector_type(2)));
typedef float f32x4 __attribute__((ext_vector_type(4)));
typedef float f32x2 __attribute__((ext_vector_type(2)));
typedef __bf16 bf16x2 __attribute__((ext_vector_type(2)));
#define GAS __attribute__((address_space(1)))

constexpr int D = 640, NH = 32, SK = 4096, DH = 128, FF = 1728;
constexpr int NWG = 256, NT = 1024;
constexpr unsigned SENT = 0xFFFFFFFFu;
constexpr unsigned SPIN_CAP = 1u << 22;

constexpr int WS_Y = 0;
constexpr int WS_ML = WS_Y + 256 * 2560;
constexpr int WS_H = WS_ML + 2048;
constexpr int WS_HH = WS_H + 5120;
constexpr int WS_END = WS_HH + 6912;

constexpr int L_PART = 0;
constexpr int L_QPK = L_PART + 2048;
constexpr int L_WML = L_QPK + 64;
constexpr int L_OPK = L_WML + 32;
constexpr int L_YP = L_OPK + 96;
constexpr int L_RP = L_YP + 12 * 640;
constexpr int L_GP = L_RP + 256;
constexpr int L_CP = L_GP + 160;
constexpr int L_SP = L_CP + 80;
constexpr int L_DP = L_SP + 32;
constexpr int L_TW = L_DP + 14 * 32;
constexpr int L_END = L_TW + 4 * 4096;

__device__ __forceinline__ float dot2(unsigned a, unsigned b, float c) {
  return __builtin_amdgcn_fdot2_f32_bf16(__builtin_bit_cast(bf16x2, a), __builtin_bit_cast(bf16x2, b), c, false);
}
__device__ __forceinline__ unsigned cvtpk(float lo, float hi) {
  bf16x2 v = {(__bf16)lo, (__bf16)hi};
  return __builtin_bit_cast(unsigned, v);
}
__device__ __forceinline__ float bflo(unsigned u) { return __builtin_bit_cast(float, u << 16); }
__device__ __forceinline__ float bfhi(unsigned u) { return __builtin_bit_cast(float, u & 0xffff0000u); }
__device__ __forceinline__ float bfr(float f) { return bflo(cvtpk(f, f)); }
__device__ __forceinline__ unsigned plo(unsigned a, unsigned b) { return __builtin_amdgcn_perm(b, a, 0x05040100u); }
__device__ __forceinline__ unsigned phi(unsigned a, unsigned b) { return __builtin_amdgcn_perm(b, a, 0x07060302u); }
template <int CTRL> __device__ __forceinline__ float dppf(float x) {
  return __builtin_bit_cast(float, __builtin_amdgcn_mov_dpp(__builtin_bit_cast(int, x), CTRL, 0xf, 0xf, true));
}
template <int CTRL> __device__ __forceinline__ unsigned dppu(unsigned x) {
  return (unsigned)__builtin_amdgcn_mov_dpp((int)x, CTRL, 0xf, 0xf, true);
}
constexpr int X1 = 0xB1, X2 = 0x4E, X7 = 0x141, X8 = 0x128, ROR4 = 0x124, ROR2 = 0x122, ROR1 = 0x121, BC = 0x150;
__device__ __forceinline__ float tr32(float a, float b) {
  auto s = __builtin_amdgcn_permlane32_swap(__float_as_uint(a), __float_as_uint(b), false, false);
  return __uint_as_float(s[0]) + __uint_as_float(s[1]);
}
__device__ __forceinline__ float tr16(float a, float b) {
  auto s = __builtin_amdgcn_permlane16_swap(__float_as_uint(a), __float_as_uint(b), false, false);
  return __uint_as_float(s[0]) + __uint_as_float(s[1]);
}
__device__ __forceinline__ float mx32(float a) {
  auto s = __builtin_amdgcn_permlane32_swap(__float_as_uint(a), __float_as_uint(a), false, false);
  return fmaxf(__uint_as_float(s[0]), __uint_as_float(s[1]));
}
__device__ __forceinline__ float mx16(float a) {
  auto s = __builtin_amdgcn_permlane16_swap(__float_as_uint(a), __float_as_uint(a), false, false);
  return fmaxf(__uint_as_float(s[0]), __uint_as_float(s[1]));
}
template <int CTRL> __device__ __forceinline__ float trd(float a, float b, bool sel) {
  const float t1 = a + dppf<CTRL>(a), t2 = b + dppf<CTRL>(b);
  return sel ? t2 : t1;
}
__device__ __forceinline__ float row_sum(float x) {
  x += dppf<X8>(x); x += dppf<ROR4>(x); x += dppf<X2>(x); x += dppf<X1>(x); return x;
}
__device__ __forceinline__ float row_max(float x) {
  x = fmaxf(x, dppf<X8>(x)); x = fmaxf(x, dppf<ROR4>(x)); x = fmaxf(x, dppf<X2>(x)); x = fmaxf(x, dppf<X1>(x)); return x;
}
__device__ __forceinline__ float wave_sum(float x) { x = row_sum(x); x = tr16(x, x); return tr32(x, x); }
__device__ __forceinline__ float wave_max(float x) { x = row_max(x); x = mx16(x); return mx32(x); }
__device__ __forceinline__ void wg_barrier() { asm volatile("s_waitcnt lgkmcnt(0)\n\ts_barrier" ::: "memory"); }
__device__ __forceinline__ unsigned canon(unsigned u) { return u == SENT ? 0x7FC07FC0u : u; }
__device__ __forceinline__ unsigned canonf(float f) { return canon(__float_as_uint(f)); }
#define SB() __builtin_amdgcn_sched_barrier(0)

constexpr int SC1 = 16, NTP = 2;
constexpr int OOB = 0x40000000;
typedef __amdgpu_buffer_rsrc_t rsrc_t;
__device__ __forceinline__ rsrc_t mk_rsrc(const GAS char* p, int bytes) {
  return __builtin_amdgcn_make_buffer_rsrc((void*)p, 0, bytes, 0x00020000);
}
template <int AUX> __device__ __forceinline__ u32x4 bld(rsrc_t r, int voff, int soff) {
  return __builtin_amdgcn_raw_buffer_load_b128(r, voff, soff, AUX);
}
#define PIN(x) asm volatile("" : "+v"(x))

__device__ __forceinline__ bool valid4(u32x4 v) { return v.x != SENT && v.y != SENT && v.z != SENT && v.w != SENT; }
__device__ __forceinline__ u32x4 poll16(__amdgpu_buffer_rsrc_t rs, int off, bool act) {
  u32x4 v;
  for (unsigned spins = 0;; ++spins) {
    asm volatile("" ::: "memory");
    v = __builtin_amdgcn_raw_buffer_load_b128(rs, off, 0, SC1);
    if (__all(valid4(v) || !act) || spins > SPIN_CAP) break;
  }
  asm volatile("" ::: "memory");
  return v;
}

__global__ void __launch_bounds__(NT, 4)
fused_decode(const GAS char* __restrict__ x_, const GAS char* __restrict__ kc, const GAS char* __restrict__ vc,
             const GAS char* __restrict__ wq, const GAS char* __restrict__ wo, const GAS char* __restrict__ wg,
             const GAS char* __restrict__ wu, const GAS char* __restrict__ wd, GAS char* __restrict__ out, char* ws) {
  __shared__ __attribute__((aligned(16))) float lds[L_END];
  const int tid = threadIdx.x, lane = tid & 63, w = __builtin_amdgcn_readfirstlane(tid >> 6);
  const int r = lane >> 4, c = lane & 15;
  const int bid = blockIdx.x, xcd = bid & 7, idx = bid >> 3;
  const int head = xcd * 4 + (idx >> 3), b = (idx >> 2) & 1, s = idx & 3;
  const __amdgpu_buffer_rsrc_t rs = __builtin_amdgcn_make_buffer_rsrc((void*)ws, 0, WS_END, 0x00020000);
  const bool roleG = idx < 27;
  const int gj = xcd * 27 + idx;
  const int rr = xcd * 5 + idx - 27;

  const GAS unsigned* xdw = (const GAS unsigned*)x_ + b * 320;
  unsigned xa[5], xb[5];
  const int rg4 = 4 * w + r;
#pragma unroll
  for (int k = 0; k < 5; ++k) { xa[k] = xdw[lane + 64 * k]; xb[k] = xdw[rg4 + 64 * k]; }
  u32x4 wv[12];
  {
    const rsrc_t rq = mk_rsrc(wq + head * 256, D * 8192 - head * 256);
    const int vo = (2 * rg4) * 8192 + c * 16;
#pragma unroll
    for (int k = 0; k < 5; ++k) {
      wv[2 * k] = bld<0>(rq, vo, (128 * k) * 8192);
      wv[2 * k + 1] = bld<0>(rq, vo, (128 * k + 1) * 8192);
    }
  }
  const size_t kvoff = ((size_t)((b * NH + head) * SK + s * 1024 + w * 64)) * 256;
  const rsrc_t rk = mk_rsrc(kc + kvoff, 16384), rv = mk_rsrc(vc + kvoff, 16384);
  const int l16 = lane * 16;
#define LDKV(R, t) bld<NTP>(R, l16, (t) * 1024)
  u32x4 ring[8];
#pragma unroll
  for (int t = 0; t < 8; ++t) ring[t] = LDKV(rk, t);
  u32x4 tw[4];
  {
    const int mat = w >= 5 ? 1 : 0, gw = w - 5 * mat;
    const int P2 = w * 32 + ((lane >> 3) << 2) + (lane & 3);
    const rsrc_t rt = mk_rsrc(roleG ? (mat ? wu : wg) : wd, D * FF * 2);
    const int stride = roleG ? FF * 2 : D * 2;
    const int vog = (128 * gw + 16 * (lane >> 3) + 4 * (lane & 3)) * (FF * 2) + gj * 16 + (w < 10 ? 0 : OOB);
    const int vod = (4 * P2) * (D * 2) + rr * 32 + ((lane >> 2) & 1) * 16;
    const int vo = roleG ? vog : vod;
#pragma unroll
    for (int e = 0; e < 4; ++e) tw[e] = bld<0>(rt, vo, e * stride);
  }
  SB();

  unsigned xnp[5];
  {
    float sm = 0.f;
#pragma unroll
    for (int k = 0; k < 5; ++k) sm += bflo(xa[k]) + bfhi(xa[k]);
    const float mean = wave_sum(sm) * (1.f / D);
    float sq = 0.f;
#pragma unroll
    for (int k = 0; k < 5; ++k) { const float a = bflo(xa[k]) - mean, bb = bfhi(xa[k]) - mean; sq += a * a + bb * bb; }
    const float var = wave_sum(sq) * (1.f / D);
    const float mb = bfr(mean), den = bfr(sqrtf(bfr(bfr(var) + 1e-5f)));
#pragma unroll
    for (int k = 0; k < 5; ++k) xnp[k] = cvtpk(bfr(bflo(xb[k]) - mb) / den, bfr(bfhi(xb[k]) - mb) / den);
  }
  {
    float a[8];
#pragma unroll
    for (int i = 0; i < 8; ++i) a[i] = 0.f;
#pragma unroll
    for (int k = 0; k < 5; ++k)
#pragma unroll
      for (int j = 0; j < 4; ++j) {
        a[2 * j] = dot2(plo(wv[2 * k][j], wv[2 * k + 1][j]), xnp[k], a[2 * j]);
        a[2 * j + 1] = dot2(phi(wv[2 * k][j], wv[2 * k + 1][j]), xnp[k], a[2 * j + 1]);
      }
    float s4[4];
#pragma unroll
    for (int i = 0; i < 4; ++i) s4[i] = tr32(a[i], a[i + 4]);
    f32x2 u = {tr16(s4[0], s4[2]), tr16(s4[1], s4[3])};
    *(f32x2*)&lds[L_PART + w * 128 + 8 * c + 2 * r] = u;
  }
  SB();
  SB();
  wg_barrier();
  if (tid < 64) {
    f32x2 q2 = {0.f, 0.f};
#pragma unroll
    for (int ww = 0; ww < 16; ++ww) q2 += *(const f32x2*)&lds[L_PART + ww * 128 + 2 * tid];
    ((unsigned*)lds)[L_QPK + tid] = cvtpk(q2.x, q2.y);
  }
  wg_barrier();
  const u32x4 qv = *(const u32x4*)&lds[L_QPK + 4 * c];

  float sc;
  {
    float l1[2], l2[2], l3[2], l4[2];
#pragma unroll
    for (int t = 0; t < 16; ++t) {
      const u32x4 kk = ring[t & 7];
      float d = dot2(kk[0], qv[0], 0.f);
      d = dot2(kk[1], qv[1], d); d = dot2(kk[2], qv[2], d); d = dot2(kk[3], qv[3], d);
      if (t < 8) ring[t & 7] = LDKV(rk, t + 8); else ring[t & 7] = LDKV(rv, t - 8);
      l1[t & 1] = d;
      if ((t & 1) == 1) l2[(t >> 1) & 1] = trd<X8>(l1[0], l1[1], (c & 8) != 0);
      if ((t & 3) == 3) l3[(t >> 2) & 1] = trd<X7>(l2[0], l2[1], (c & 4) != 0);
      if ((t & 7) == 7) l4[(t >> 3) & 1] = trd<X2>(l3[0], l3[1], (c & 2) != 0);
      SB();
      if (t == 7) {
#pragma unroll
        for (int i = 0; i < 2; ++i) {
          u32x4 lo, hi;
#pragma unroll
          for (int j = 0; j < 4; ++j) { lo[j] = plo(tw[2 * i][j], tw[2 * i + 1][j]); hi[j] = phi(tw[2 * i][j], tw[2 * i + 1][j]); }
          *(u32x4*)&lds[L_TW + (2 * i) * 4096 + tid * 4] = lo;
          *(u32x4*)&lds[L_TW + (2 * i + 1) * 4096 + tid * 4] = hi;
        }
        SB();
        const int org = tid / 80, occ = tid - org * 80;
        const rsrc_t ro = mk_rsrc(wo + (size_t)head * (128 * 1280), 128 * 1280);
        const int vo = (2 * org) * 1280 + occ * 16 + (org < 12 ? 0 : OOB);
#pragma unroll
        for (int i = 0; i < 6; ++i) {
          wv[2 * i] = bld<0>(ro, vo, i * 24 * 1280);
          wv[2 * i + 1] = bld<0>(ro, vo, i * 24 * 1280 + 1280);
        }
        SB();
      }
    }
    sc = trd<X1>(l4[0], l4[1], (c & 1) != 0);
  }
  const float s2 = bfr(bfr(sc) * 0.08838834764831845f) * 1.4426950408889634f;
  {
    const float mw = wave_max(s2);
    const float lw = wave_sum(__builtin_amdgcn_exp2f(s2 - mw));
    if (lane == 0) { f32x2 t = {mw, lw}; *(f32x2*)&lds[L_WML + 2 * w] = t; }
  }
  wg_barrier();
  float Mwg;
  unsigned ppk;
  {
    const f32x2 ml = *(const f32x2*)&lds[L_WML + 2 * c];
    Mwg = row_max(ml.x);
    const float p = __builtin_amdgcn_exp2f(s2 - Mwg);
    ppk = cvtpk(p, dppf<X8>(p));
    if (w == 0) {
      const float Lwg = row_sum(ml.y * __builtin_amdgcn_exp2f(ml.x - Mwg));
      if (lane == 0) {
        u32x2 o = {canonf(Mwg), canonf(Lwg)};
        __builtin_amdgcn_raw_buffer_store_b64(o, rs, WS_ML + ((b * NH + head) * 4 + s) * 8, 0, SC1);
      }
    }
  }
  const int mloff = WS_ML + ((b * NH + head) * 4 + (lane & 3)) * 8;
  u32x2 mls;
  {
    float a[8];
#pragma unroll
    for (int i = 0; i < 8; ++i) a[i] = 0.f;
#pragma unroll
    for (int u = 0; u < 8; ++u) {
      const u32x4 v0 = ring[(2 * u) & 7], v1 = ring[(2 * u + 1) & 7];
      unsigned pp;
      switch (u) {
        case 0: pp = dppu<BC + 0>(ppk); break;
        case 1: pp = dppu<BC + 4>(ppk); break;
        case 2: pp = dppu<BC + 2>(ppk); break;
        case 3: pp = dppu<BC + 6>(ppk); break;
        case 4: pp = dppu<BC + 1>(ppk); break;
        case 5: pp = dppu<BC + 5>(ppk); break;
        case 6: pp = dppu<BC + 3>(ppk); break;
        default: pp = dppu<BC + 7>(ppk); break;
      }
#pragma unroll
      for (int j = 0; j < 4; ++j) {
        a[2 * j] = dot2(plo(v0[j], v1[j]), pp, a[2 * j]);
        a[2 * j + 1] = dot2(phi(v0[j], v1[j]), pp, a[2 * j + 1]);
      }
      if (u < 4) {
        ring[(2 * u) & 7] = LDKV(rv, 2 * u + 8);
        ring[(2 * u + 1) & 7] = LDKV(rv, 2 * u + 9);
      }
      if (u == 3) {
#pragma unroll
        for (int i = 0; i < 6; ++i)
#pragma unroll
          for (int j = 0; j < 4; ++j) {
            const unsigned lo = plo(wv[2 * i][j], wv[2 * i + 1][j]), hi = phi(wv[2 * i][j], wv[2 * i + 1][j]);
            wv[2 * i][j] = lo; wv[2 * i + 1][j] = hi;
          }
#pragma unroll
        for (int i = 0; i < 12; ++i) PIN(wv[i]);
      }
      if (u == 4) mls = __builtin_amdgcn_raw_buffer_load_b64(rs, mloff, 0, SC1);
      SB();
    }
    float s4[4];
#pragma unroll
    for (int i = 0; i < 4; ++i) s4[i] = tr32(a[i], a[i + 4]);
    f32x2 u = {tr16(s4[0], s4[2]), tr16(s4[1], s4[3])};
    *(f32x2*)&lds[L_PART + w * 128 + 8 * c + 2 * r] = u;
  }
  float oscale = 0.f;
  if (w == 0) {
    for (unsigned spins = 0; !__all(mls.x != SENT && mls.y != SENT) && spins < SPIN_CAP; ++spins) {
      asm volatile("" ::: "memory");
      mls = __builtin_amdgcn_raw_buffer_load_b64(rs, mloff, 0, SC1);
    }
    const float ms = __uint_as_float(mls.x), ls = __uint_as_float(mls.y);
    float M = fmaxf(ms, dppf<X1>(ms)); M = fmaxf(M, dppf<X2>(M));
    float L = ls * __builtin_amdgcn_exp2f(ms - M);
    L += dppf<X1>(L); L += dppf<X2>(L);
    oscale = __builtin_amdgcn_exp2f(Mwg - M) / L;
  }
  wg_barrier();
  if (tid < 64) {
    f32x2 o2 = {0.f, 0.f};
#pragma unroll
    for (int ww = 0; ww < 16; ++ww) o2 += *(const f32x2*)&lds[L_PART + ww * 128 + 2 * tid];
    ((unsigned*)lds)[L_OPK + (tid % 12) * 8 + tid / 12] = cvtpk(o2.x * oscale, o2.y * oscale);
  } else if (tid < 72) {
    ((unsigned*)lds)[L_OPK + (tid % 12) * 8 + tid / 12] = 0u;
  }
  wg_barrier();
  if (tid < 960) {
    const int org = tid / 80;
    const u32x4 o0 = *(const u32x4*)&lds[L_OPK + org * 8];
    const u32x2 o1 = *(const u32x2*)&lds[L_OPK + org * 8 + 4];
    const unsigned op[6] = {o0.x, o0.y, o0.z, o0.w, o1.x, o1.y};
    f32x4 y0 = {0.f, 0.f, 0.f, 0.f}, y1 = {0.f, 0.f, 0.f, 0.f};
#pragma unroll
    for (int i = 0; i < 6; ++i) {
      y0.x = dot2(wv[2 * i][0], op[i], y0.x); y0.y = dot2(wv[2 * i + 1][0], op[i], y0.y);
      y0.z = dot2(wv[2 * i][1], op[i], y0.z); y0.w = dot2(wv[2 * i + 1][1], op[i], y0.w);
      y1.x = dot2(wv[2 * i][2], op[i], y1.x); y1.y = dot2(wv[2 * i + 1][2], op[i], y1.y);
      y1.z = dot2(wv[2 * i][3], op[i], y1.z); y1.w = dot2(wv[2 * i + 1][3], op[i], y1.w);
    }
    *(f32x4*)&lds[L_YP + tid * 8] = y0;
    *(f32x4*)&lds[L_YP + tid * 8 + 4] = y1;
  }
  wg_barrier();
  if (tid < 160) {
    f32x4 y = {0.f, 0.f, 0.f, 0.f};
#pragma unroll
    for (int g = 0; g < 12; ++g) y += *(const f32x4*)&lds[L_YP + g * 640 + 4 * tid];
    u32x4 o = {canonf(y.x), canonf(y.y), canonf(y.z), canonf(y.w)};
    __builtin_amdgcn_raw_buffer_store_b128(o, rs, WS_Y + (b * 128 + head * 4 + s) * 2560 + tid * 16, 0, SC1);
  }
#pragma unroll
  for (int e = 0; e < 4; ++e) tw[e] = *(const u32x4*)&lds[L_TW + e * 4096 + tid * 4];

  if (roleG) {
    if (w < 10) {
      const int mat = w >= 5 ? 1 : 0, gw = w - 5 * mat;
      {
        const unsigned ones = 0x3F803F80u;
        float v[8];
#pragma unroll
        for (int j = 0; j < 4; ++j) {
          v[2 * j] = dot2(tw[2][j], ones, dot2(tw[0][j], ones, 0.f));
          v[2 * j + 1] = dot2(tw[3][j], ones, dot2(tw[1][j], ones, 0.f));
        }
        float s4[4];
#pragma unroll
        for (int i = 0; i < 4; ++i) s4[i] = tr32(v[i], v[i + 4]);
        const float u0 = tr16(s4[0], s4[2]), u1 = tr16(s4[1], s4[3]);
        float t = trd<X8>(u0, u1, (c & 8) != 0);
        t += dppf<X1>(t); t += dppf<X2>(t);
        if ((lane & 7) == 0) lds[L_CP + w * 8 + 2 * r + (c >> 3)] = t;
      }
      const u32x4 hv = poll16(rs, WS_H + (gw * 64 + lane) * 16, true);
      const unsigned h01 = phi(hv.x, hv.y), h23 = phi(hv.z, hv.w);
      float v[8];
#pragma unroll
      for (int j = 0; j < 4; ++j) {
        v[2 * j] = dot2(tw[2][j], h23, dot2(tw[0][j], h01, 0.f));
        v[2 * j + 1] = dot2(tw[3][j], h23, dot2(tw[1][j], h01, 0.f));
      }
      float s4[4];
#pragma unroll
      for (int i = 0; i < 4; ++i) s4[i] = tr32(v[i], v[i + 4]);
      const float u0 = tr16(s4[0], s4[2]), u1 = tr16(s4[1], s4[3]);
      float t = trd<X8>(u0, u1, (c & 8) != 0);
      t += dppf<X1>(t); t += dppf<X2>(t);
      if ((lane & 3) == 0) lds[L_GP + w * 16 + ((lane >> 2) & 1) * 8 + 2 * r + (c >> 3)] = t;
      if (mat == 0) {
        const float h0 = __uint_as_float(hv.x), h1 = __uint_as_float(hv.y), h2 = __uint_as_float(hv.z), h3 = __uint_as_float(hv.w);
        float sm = (h0 + h1) + (h2 + h3);
        sm += dppf<X1>(sm); sm += dppf<X2>(sm); sm += dppf<X8>(sm); sm = tr16(sm, sm); sm = tr32(sm, sm);
        const float mw = sm * (1.f / 128.f);
        const float d0 = h0 - mw, d1 = h1 - mw, d2 = h2 - mw, d3 = h3 - mw;
        float sq = (d0 * d0 + d1 * d1) + (d2 * d2 + d3 * d3);
        sq += dppf<X1>(sq); sq += dppf<X2>(sq); sq += dppf<X8>(sq); sq = tr16(sq, sq); sq = tr32(sq, sq);
        if ((lane & 59) == 0) { f32x2 t2 = {mw, sq}; *(f32x2*)&lds[L_SP + gw * 4 + (lane >> 2) * 2] = t2; }
      }
    }
    wg_barrier();
    if (tid < 4) {
      const int bb = tid & 1, e = tid >> 1;
      f32x4 g = {0.f, 0.f, 0.f, 0.f}, u = g, cg = g, cu = g;
      float mws[5], m2 = 0.f, mean = 0.f;
#pragma unroll
      for (int i = 0; i < 5; ++i) {
        g += *(const f32x4*)&lds[L_GP + i * 16 + bb * 8 + 4 * e];
        u += *(const f32x4*)&lds[L_GP + (5 + i) * 16 + bb * 8 + 4 * e];
        cg += *(const f32x4*)&lds[L_CP + i * 8 + 4 * e];
        cu += *(const f32x4*)&lds[L_CP + (5 + i) * 8 + 4 * e];
        const f32x2 st = *(const f32x2*)&lds[L_SP + i * 4 + bb * 2];
        mws[i] = st.x; mean += st.x; m2 += st.y;
      }
      mean *= 0.2f;
#pragma unroll
      for (int i = 0; i < 5; ++i) { const float dd = mws[i] - mean; m2 += 128.f * dd * dd; }
      const float var = m2 * (1.f / D);
      const float mb = bfr(mean), rstd = 1.f / bfr(sqrtf(bfr(bfr(var) + 1e-5f)));
      float hh[4];
#pragma unroll
      for (int k = 0; k < 4; ++k) {
        float gg = (g[k] - mb * cg[k]) * rstd, uu = (u[k] - mb * cu[k]) * rstd;
        gg = bfr(gg); uu = bfr(uu);
        hh[k] = gg * uu;
      }
      u32x2 o = {canon(cvtpk(hh[0], hh[1])), canon(cvtpk(hh[2], hh[3]))};
      __builtin_amdgcn_raw_buffer_store_b64(o, rs, WS_HH + (2 * gj + e) * 16 + bb * 8, 0, SC1);
    }
  } else {
    u32x2 xr = {0u, 0u};
    if (tid < 8) xr = *(const GAS u32x2*)(x_ + (tid >> 2) * (D * 2) + rr * 32 + (tid & 3) * 8);
    {
      const u32x4 yv = poll16(rs, WS_Y + (tid >> 2) * 2560 + rr * 64 + (tid & 3) * 16, true);
      const float s0 = tr32(__uint_as_float(yv.x), __uint_as_float(yv.z)), s1 = tr32(__uint_as_float(yv.y), __uint_as_float(yv.w));
      float u = tr16(s0, s1);
      u += dppf<X8>(u); u += dppf<ROR4>(u);
      if (c < 4) lds[L_RP + w * 16 + 4 * c + r] = u;
    }
    wg_barrier();
    f32x4 hres = {0.f, 0.f, 0.f, 0.f};
    if (tid < 8) {
      const int bb = tid >> 2, q4 = tid & 3;
      f32x4 a = {0.f, 0.f, 0.f, 0.f};
#pragma unroll
      for (int i = 0; i < 8; ++i) a += *(const f32x4*)&lds[L_RP + (8 * bb + i) * 16 + 4 * q4];
      a.x = bfr(a.x); a.y = bfr(a.y); a.z = bfr(a.z); a.w = bfr(a.w);
      hres.x = bfr(bflo(xr.x) + a.x); hres.y = bfr(bfhi(xr.x) + a.y);
      hres.z = bfr(bflo(xr.y) + a.z); hres.w = bfr(bfhi(xr.y) + a.w);
      u32x4 o = {canonf(hres.x), canonf(hres.y), canonf(hres.z), canonf(hres.w)};
      __builtin_amdgcn_raw_buffer_store_b128(o, rs, WS_H + rr * 128 + tid * 16, 0, SC1);
    }
    if (w < 14) {
      const int P2 = w * 32 + ((lane >> 3) << 2) + (lane & 3);
      const bool tact = P2 < 432;
      u32x4 hv = poll16(rs, WS_HH + (tact ? P2 : 0) * 16, tact);
      if (!tact) { u32x4 z = {0u, 0u, 0u, 0u}; hv = z; }
      float v[16];
#pragma unroll
      for (int j = 0; j < 4; ++j) {
        v[2 * j] = dot2(tw[2][j], hv.y, dot2(tw[0][j], hv.x, 0.f));
        v[2 * j + 1] = dot2(tw[3][j], hv.y, dot2(tw[1][j], hv.x, 0.f));
        v[8 + 2 * j] = dot2(tw[2][j], hv.w, dot2(tw[0][j], hv.z, 0.f));
        v[8 + 2 * j + 1] = dot2(tw[3][j], hv.w, dot2(tw[1][j], hv.z, 0.f));
      }
      float s8[8], s4[4];
#pragma unroll
      for (int i = 0; i < 8; ++i) s8[i] = tr32(v[i], v[i + 8]);
#pragma unroll
      for (int i = 0; i < 4; ++i) s4[i] = tr16(s8[i], s8[i + 4]);
      const float t0 = trd<X8>(s4[0], s4[2], (c & 8) != 0), t1 = trd<X8>(s4[1], s4[3], (c & 8) != 0);
      float t = trd<X2>(t0, t1, (c & 2) != 0);
      t += dppf<X1>(t);
      if ((lane & 1) == 0)
        lds[L_DP + w * 32 + (lane >> 5) * 16 + ((lane >> 2) & 1) * 8 + ((lane >> 4) & 1) * 4 + ((lane >> 3) & 1) * 2 + ((lane >> 1) & 1)] = t;
    }
    wg_barrier();
    if (tid < 8) {
      const int bb = tid >> 2, q4 = tid & 3;
      f32x4 a = {0.f, 0.f, 0.f, 0.f};
#pragma unroll
      for (int i = 0; i < 14; ++i) a += *(const f32x4*)&lds[L_DP + i * 32 + bb * 16 + 4 * q4];
      a.x = bfr(a.x); a.y = bfr(a.y); a.z = bfr(a.z); a.w = bfr(a.w);
      u32x2 o = {cvtpk(hres.x + a.x, hres.y + a.y), cvtpk(hres.z + a.z, hres.w + a.w)};
      *(GAS u32x2*)(out + bb * (D * 2) + rr * 32 + q4 * 8) = o;
    }
  }
}

extern "C" void kernel_launch(void* const* d_in, const int* in_sizes, int n_in, void* d_out, int out_size, void* d_ws,
                              size_t ws_size, hipStream_t stream) {
  (void)in_sizes; (void)n_in; (void)out_size; (void)ws_size;
  (void)hipMemsetAsync(d_ws, 0xFF, WS_END, stream);
  fused_decode<<<NWG, NT, 0, stream>>>((const GAS char*)d_in[0], (const GAS char*)d_in[1], (const GAS char*)d_in[2],
                                       (const GAS char*)d_in[3], (const GAS char*)d_in[4], (const GAS char*)d_in[6],
                                       (const GAS char*)d_in[7], (const GAS char*)d_in[8], (GAS char*)d_out, (char*)d_ws);
}
```

```cpp
#include <hip/hip_runtime.h>
#include <stdint.h>

#define MIMIC 1

typedef unsigned u32x4 __attribute__((ext_vector_type(4)));
typedef unsigned u32x2 __attribute__((ext_vector_type(2)));
typedef float f32x4 __attribute__((ext_vector_type(4)));
typedef float f32x2 __attribute__((ext_vector_type(2)));
typedef __bf16 bf16x2 __attribute__((ext_vector_type(2)));
#define GAS __attribute__((address_space(1)))
#define LAS __attribute__((address_space(3)))
#define WAUX 2
#define PREF 1
#define ABL 0
#define PRIO 1

constexpr int D = 640, NH = 32, SK = 4096, DH = 128, FF = 1728;
constexpr int NWG = 256, NT = 1024;
constexpr unsigned SPIN_CAP = 1u << 22;

constexpr int WS_EPOCH = 0;
constexpr int WS_QX = 256;
constexpr int WS_ML = WS_QX + 256 * 2048;
constexpr int WS_OX = WS_ML + 4096;
constexpr int WS_Y = WS_OX + 256 * 1024;
constexpr int GP_REC = 17 * 128;
constexpr int WS_GP = WS_Y + 80 * 4096;
constexpr int WS_CS = WS_GP + 216 * GP_REC;
constexpr int WS_OP = WS_CS + 216 * 1024;
constexpr int WS_END = WS_OP + 80 * 4096;
static_assert(WS_OX % 128 == 0 && WS_Y % 128 == 0 && WS_GP % 128 == 0 && WS_OP % 128 == 0, "line alignment");

constexpr int L_PART = 0;
constexpr int L_QPK = L_PART + 2560;
constexpr int L_WML = L_QPK + 64;
constexpr int L_CTL = L_WML + 32;
constexpr int L_OPK = L_CTL + 8;
constexpr int L_ST = L_OPK + 128;
constexpr int L_G2 = L_ST + 40;
constexpr int L_CSP = L_G2 + 272;
constexpr int L_CS = L_CSP + 1280;
constexpr int L_WO = L_CS + 128;
constexpr int L_WD = L_WO + 5120;
constexpr int L_END = L_WD + 2560;

__device__ __forceinline__ float dot2(unsigned a, unsigned b, float c) {
  return __builtin_amdgcn_fdot2_f32_bf16(__builtin_bit_cast(bf16x2, a), __builtin_bit_cast(bf16x2, b), c, false);
}
__device__ __forceinline__ unsigned cvtpk(float lo, float hi) {
  bf16x2 v = {(__bf16)lo, (__bf16)hi};
  return __builtin_bit_cast(unsigned, v);
}
__device__ __forceinline__ float bflo(unsigned u) { return __builtin_bit_cast(float, u << 16); }
__device__ __forceinline__ float bfhi(unsigned u) { return __builtin_bit_cast(float, u & 0xffff0000u); }
__device__ __forceinline__ float bfr(float f) { return bflo(cvtpk(f, f)); }
__device__ __forceinline__ unsigned plo(unsigned a, unsigned b) { return __builtin_amdgcn_perm(b, a, 0x05040100u); }
__device__ __forceinline__ unsigned phi(unsigned a, unsigned b) { return __builtin_amdgcn_perm(b, a, 0x07060302u); }
template <int CTRL> __device__ __forceinline__ float dppf(float x) {
  return __builtin_bit_cast(float, __builtin_amdgcn_mov_dpp(__builtin_bit_cast(int, x), CTRL, 0xf, 0xf, true));
}
template <int CTRL> __device__ __forceinline__ unsigned dppu(unsigned x) {
  return (unsigned)__builtin_amdgcn_mov_dpp((int)x, CTRL, 0xf, 0xf, true);
}
constexpr int X1 = 0xB1, X2 = 0x4E, X7 = 0x141, X8 = 0x128, ROR4 = 0x124, ROR2 = 0x122, ROR1 = 0x121, BC = 0x150;
__device__ __forceinline__ float tr32(float a, float b) {
  auto s = __builtin_amdgcn_permlane32_swap(__float_as_uint(a), __float_as_uint(b), false, false);
  return __uint_as_float(s[0]) + __uint_as_float(s[1]);
}
__device__ __forceinline__ float tr16(float a, float b) {
  auto s = __builtin_amdgcn_permlane16_swap(__float_as_uint(a), __float_as_uint(b), false, false);
  return __uint_as_float(s[0]) + __uint_as_float(s[1]);
}
__device__ __forceinline__ float mx32(float a) {
  auto s = __builtin_amdgcn_permlane32_swap(__float_as_uint(a), __float_as_uint(a), false, false);
  return fmaxf(__uint_as_float(s[0]), __uint_as_float(s[1]));
}
__device__ __forceinline__ float mx16(float a) {
  auto s = __builtin_amdgcn_permlane16_swap(__float_as_uint(a), __float_as_uint(a), false, false);
  return fmaxf(__uint_as_float(s[0]), __uint_as_float(s[1]));
}
template <int CTRL> __device__ __forceinline__ float trd(float a, float b, bool sel) {
  const float t1 = a + dppf<CTRL>(a), t2 = b + dppf<CTRL>(b);
  return sel ? t2 : t1;
}
__device__ __forceinline__ float row_sum(float x) {
  x += dppf<X8>(x); x += dppf<ROR4>(x); x += dppf<X2>(x); x += dppf<X1>(x); return x;
}
__device__ __forceinline__ float row_max(float x) {
  x = fmaxf(x, dppf<X8>(x)); x = fmaxf(x, dppf<ROR4>(x)); x = fmaxf(x, dppf<X2>(x)); x = fmaxf(x, dppf<X1>(x)); return x;
}
__device__ __forceinline__ float wave_sum(float x) { x = row_sum(x); x = tr16(x, x); return tr32(x, x); }
__device__ __forceinline__ float wave_max(float x) { x = row_max(x); x = mx16(x); return mx32(x); }
__device__ __forceinline__ float rdl(float x, int l) { return __int_as_float(__builtin_amdgcn_readlane(__float_as_int(x), l)); }
__device__ __forceinline__ void wg_barrier() { asm volatile("s_waitcnt lgkmcnt(0)\n\ts_barrier" ::: "memory"); }
#define SB() __builtin_amdgcn_sched_barrier(0)
#define STAMP(i)
#define STAMP_DECL
#define STAMP_OUT()

constexpr int SC1 = 16, NTP = 2;
constexpr int OOB = 0x40000000;
typedef __amdgpu_buffer_rsrc_t rsrc_t;
__device__ __forceinline__ rsrc_t mk_rsrc(const GAS char* p, int bytes) {
  return __builtin_amdgcn_make_buffer_rsrc((void*)p, 0, bytes, 0x00020000);
}
template <int AUX> __device__ __forceinline__ u32x4 bld(rsrc_t r, int voff, int soff) {
  return __builtin_amdgcn_raw_buffer_load_b128(r, voff, soff, AUX);
}
#define PIN(x) asm volatile("" : "+v"(x))

__device__ __forceinline__ bool tagok(u32x4 v, unsigned t0, unsigned t1) { return v.y == t0 && v.w == t1; }
__device__ __forceinline__ u32x4 gld(rsrc_t rs, int voff, int soff) { return __builtin_amdgcn_raw_buffer_load_b128(rs, voff, soff, SC1); }
__device__ __forceinline__ void gst(rsrc_t rs, int voff, unsigned a, unsigned b, unsigned t0, unsigned t1) {
  const u32x4 g = {a, t0, b, t1};
  __builtin_amdgcn_raw_buffer_store_b128(g, rs, voff, 0, SC1);
}
#define FENCE() asm volatile("" ::: "memory")
#define PSLEEP 2
template <int N> __device__ __forceinline__ void gather(rsrc_t rs, int (&vo)[N], f32x2 (&v)[N], unsigned t0, unsigned t1) {
#pragma unroll
  for (int i = 0; i < N; ++i) { v[i].x = 0.f; v[i].y = 0.f; }
  for (unsigned spins = 0;; ++spins) {
    FENCE();
    u32x4 g[N];
#pragma unroll
    for (int i = 0; i < N; ++i) g[i] = gld(rs, vo[i], 0);
    bool more = false;
#pragma unroll
    for (int i = 0; i < N; ++i) {
      const bool ok = tagok(g[i], t0, t1) && vo[i] != OOB;
      v[i].x = ok ? __uint_as_float(g[i].x) : v[i].x;
      v[i].y = ok ? __uint_as_float(g[i].z) : v[i].y;
      vo[i] = ok ? OOB : vo[i];
      more = more || vo[i] != OOB;
    }
    if (!__any(more) || spins > SPIN_CAP) break;
    if (PSLEEP) __builtin_amdgcn_s_sleep(PSLEEP);
  }
  FENCE();
}

__global__ void __launch_bounds__(NT, 4)
fused_decode(const GAS char* __restrict__ x_, const GAS char* __restrict__ kc, const GAS char* __restrict__ vc,
             const GAS char* __restrict__ wq, const GAS char* __restrict__ wo, const GAS char* __restrict__ wg,
             const GAS char* __restrict__ wu, const GAS char* __restrict__ wd, GAS char* __restrict__ out, char* ws) {
  __shared__ __attribute__((aligned(16))) float lds[L_END];
  STAMP_DECL;
  STAMP(0);
  int tid = threadIdx.x, lane = tid & 63;
  const int w = __builtin_amdgcn_readfirstlane(tid >> 6);
  int r = lane >> 4, c = lane & 15;
#define REID() do { asm volatile("" : "+v"(tid)); lane = tid & 63; r = lane >> 4; c = lane & 15; } while (0)
  const int bid = blockIdx.x, xcd = bid & 7, idx = bid >> 3;
  const int head = xcd * 4 + (idx >> 3), b = (idx >> 2) & 1, s = idx & 3;
  const rsrc_t rs = __builtin_amdgcn_make_buffer_rsrc((void*)ws, 0, WS_END, 0x00020000);
  const bool roleG = idx < 27;
  const int sub = idx & 7, lid = head * 8 + sub;
  volatile LAS unsigned* ctl = (volatile LAS unsigned*)&lds[L_CTL];
  const int l16 = lane * 16;

  if (tid < 4) ctl[tid] = 0u;
  __builtin_amdgcn_s_setprio(3);
  wg_barrier();
  if (w < 10) {
    const GAS unsigned* xdw = (const GAS unsigned*)x_;
    const int P = 40 * sub + 4 * w + r;
    const rsrc_t rq = mk_rsrc(wq + head * 256, (ABL & 1) ? 0 : D * 8192 - head * 256);
    unsigned xa[10];
#pragma unroll
    for (int k = 0; k < 10; ++k) xa[k] = xdw[(lane >> 5) * 320 + (lane & 31) + 32 * k];
    const unsigned xb0 = xdw[P], xb1 = xdw[320 + P];
    const u32x4 wq0 = bld<WAUX>(rq, (2 * P) * 8192 + c * 16, 0);
    const u32x4 wq1 = bld<WAUX>(rq, (2 * P) * 8192 + c * 16, 8192);
    const u32x2 ep = __builtin_amdgcn_raw_buffer_load_b64(rs, w == 0 ? WS_EPOCH : OOB, 0, SC1);
    SB();
    STAMP(1);
    const unsigned ones = 0x3F803F80u;
    float sm = 0.f;
#pragma unroll
    for (int k = 0; k < 10; ++k) sm = dot2(xa[k], ones, sm);
    sm = row_sum(sm); sm = tr16(sm, sm);
    const float mean = sm * (1.f / D);
    float sq = 0.f, sq2 = 0.f;
#pragma unroll
    for (int k = 0; k < 10; ++k) { const float a = bflo(xa[k]) - mean, bb = bfhi(xa[k]) - mean; sq = fmaf(a, a, sq); sq2 = fmaf(bb, bb, sq2); }
    sq += sq2;
    sq = row_sum(sq); sq = tr16(sq, sq);
    const float var = sq * (1.f / D);
    const float mb = bfr(mean), rstd = __builtin_amdgcn_rcpf(bfr(__builtin_amdgcn_sqrtf(bfr(bfr(var) + 1e-5f))));
    const float m0 = rdl(mb, 0), m1 = rdl(mb, 32), d0 = rdl(rstd, 0), d1 = rdl(rstd, 32);
    const unsigned xn0 = cvtpk(bfr(bflo(xb0) - m0) * d0, bfr(bfhi(xb0) - m0) * d0);
    const unsigned xn1 = cvtpk(bfr(bflo(xb1) - m1) * d1, bfr(bfhi(xb1) - m1) * d1);
    float a0[8], a1[8];
#pragma unroll
    for (int j = 0; j < 4; ++j) {
      const unsigned lo = plo(wq0[j], wq1[j]), hi = phi(wq0[j], wq1[j]);
      a0[2 * j] = dot2(lo, xn0, 0.f); a0[2 * j + 1] = dot2(hi, xn0, 0.f);
      a1[2 * j] = dot2(lo, xn1, 0.f); a1[2 * j + 1] = dot2(hi, xn1, 0.f);
    }
    float s0[4], s1[4];
#pragma unroll
    for (int i = 0; i < 4; ++i) { s0[i] = tr32(a0[i], a0[i + 4]); s1[i] = tr32(a1[i], a1[i + 4]); }
    f32x2 u0 = {tr16(s0[0], s0[2]), tr16(s0[1], s0[3])}, u1 = {tr16(s1[0], s1[2]), tr16(s1[1], s1[3])};
    *(f32x2*)&lds[L_PART + w * 256 + 8 * c + 2 * r] = u0;
    *(f32x2*)&lds[L_PART + w * 256 + 128 + 8 * c + 2 * r] = u1;
    FENCE();
    if (lane == 0) __hip_atomic_fetch_add((LAS unsigned*)&ctl[0], 1u, __ATOMIC_RELAXED, __HIP_MEMORY_SCOPE_WORKGROUP);
    STAMP(2);
    if (w == 0) {
      const unsigned long long e64 = ((unsigned long long)ep.y << 32) | ep.x;
      const unsigned long long tg = e64 * 6364136223846793005ull + 1442695040888963407ull;
      const unsigned t0 = (unsigned)tg, t1 = (unsigned)(tg >> 32);
      if (lane == 0) { ctl[4] = t0; ctl[5] = t1; }
      while (ctl[0] < 10u) {}
      FENCE();
      f32x2 p0 = {0.f, 0.f}, p1 = {0.f, 0.f};
#pragma unroll
      for (int ww = 0; ww < 10; ++ww) {
        p0 += *(const f32x2*)&lds[L_PART + ww * 256 + 2 * lane];
        p1 += *(const f32x2*)&lds[L_PART + ww * 256 + 128 + 2 * lane];
      }
      gst(rs, WS_QX + lid * 2048 + l16, __float_as_uint(p0.x), __float_as_uint(p0.y), t0, t1);
      gst(rs, WS_QX + lid * 2048 + 1024 + l16, __float_as_uint(p1.x), __float_as_uint(p1.y), t0, t1);
      STAMP(3);
      f32x2 qs;
      for (unsigned spins = 0;; ++spins) {
        FENCE();
        u32x4 qq[8];
#pragma unroll
        for (int k = 0; k < 8; ++k) qq[k] = gld(rs, WS_QX + head * 16384 + b * 1024 + l16, k * 2048);
        bool ok = true;
        qs.x = 0.f; qs.y = 0.f;
#pragma unroll
        for (int k = 0; k < 8; ++k) {
          ok = ok && tagok(qq[k], t0, t1);
          qs.x += __uint_as_float(qq[k].x); qs.y += __uint_as_float(qq[k].z);
        }
        if (__all(ok) || spins > SPIN_CAP) break;
      }
      ((unsigned*)lds)[L_QPK + lane] = cvtpk(qs.x, qs.y);
      FENCE();
      if (lane == 0) ctl[1] = 1u;
    }
  }
  SB();
  REID();
  __builtin_amdgcn_s_setprio(2);
  const size_t kvoff = ((size_t)((b * NH + head) * SK + s * 1024 + w * 64)) * 256;
  const rsrc_t rk = mk_rsrc(kc + kvoff, 16384), rv = mk_rsrc(vc + kvoff, 16384);
#define LDKV(R, t) bld<NTP>(R, l16, (t) * 1024)
  u32x4 kr[16], vr[16], tw[2], gwt[2];
  {
    const int tt = w < 10 ? tid : tid - 640;
    const int Q = tt / 10, ch = tt - Q * 10;
    const bool isd = w >= 10;
    const rsrc_t rt = mk_rsrc(isd ? wd + (size_t)idx * (64 * 1280) + xcd * 160 : wo + (size_t)head * (128 * 1280) + sub * 160,
                              ((ABL & 4) || w == 15 || (isd && !roleG)) ? 0 : 128 * 1280 - 1120);
    const int vo = (2 * Q) * 1280 + ch * 16;
    const GAS char* gp = ((lane & 8) ? wu : wg) + (size_t)(80 * xcd + 8 * w + 2 * r) * (FF * 2) + idx * 128 + (lane & 7) * 16;
    const bool gact = roleG && w < 10 && !(ABL & 2);
    if (!gact) gp = wg;
#pragma unroll
    for (int t = 0; t < 16; ++t) {
      kr[t] = LDKV(rk, t);
      if (t == 2) tw[0] = bld<WAUX>(rt, vo, 0);
      if (t == 5) gwt[0] = __builtin_nontemporal_load((const GAS u32x4*)gp);
      if (t == 8) tw[1] = bld<WAUX>(rt, vo, 1280);
      if (t == 11) gwt[1] = __builtin_nontemporal_load((const GAS u32x4*)(gp + FF * 2));
      SB();
    }
  }
  while (ctl[1] == 0u) __builtin_amdgcn_s_sleep(1);
  FENCE();
  REID();
  const u32x4 qv = *(const u32x4*)&lds[L_QPK + 4 * c];
  const unsigned tag0 = __builtin_amdgcn_readfirstlane(ctl[4]), tag1 = __builtin_amdgcn_readfirstlane(ctl[5]);
  STAMP(4);

  float sc;
  {
    float l1[2], l2[2], l3[2], l4[2];
#pragma unroll
    for (int t = 0; t < 16; ++t) {
      const u32x4 kk = kr[t];
      float d = dot2(kk[0], qv[0], 0.f);
      d = dot2(kk[1], qv[1], d); d = dot2(kk[2], qv[2], d); d = dot2(kk[3], qv[3], d);
      l1[t & 1] = d;
      if ((t & 1) == 1) l2[(t >> 1) & 1] = trd<X8>(l1[0], l1[1], (c & 8) != 0);
      if ((t & 3) == 3) l3[(t >> 2) & 1] = trd<X7>(l2[0], l2[1], (c & 4) != 0);
      if ((t & 7) == 7) l4[(t >> 3) & 1] = trd<X2>(l3[0], l3[1], (c & 2) != 0);
      SB();
    }
    sc = trd<X1>(l4[0], l4[1], (c & 1) != 0);
  }
  const float s2 = bfr(bfr(sc) * 0.08838834764831845f) * 1.4426950408889634f;
  unsigned ppk;
  {
    const float mw = wave_max(s2);
    const float p = __builtin_amdgcn_exp2f(s2 - mw);
    ppk = cvtpk(p, dppf<X8>(p));
    const float lw = wave_sum(p);
    unsigned old = 0u;
    if (lane == 0) {
      f32x2 t = {mw, lw}; *(f32x2*)&lds[L_WML + 2 * w] = t;
      FENCE();
      old = __hip_atomic_fetch_add((LAS unsigned*)&ctl[2], 1u, __ATOMIC_RELAXED, __HIP_MEMORY_SCOPE_WORKGROUP);
    }
    if (__builtin_amdgcn_readfirstlane(old) == 15u) {
      FENCE();
      const f32x2 ml = *(const f32x2*)&lds[L_WML + 2 * c];
      const float Mwg = row_max(ml.x);
      const float Lwg = row_sum(ml.y * __builtin_amdgcn_exp2f(ml.x - Mwg));
      if (lane == 0) gst(rs, WS_ML + ((b * NH + head) * 4 + s) * 16, __float_as_uint(Mwg), __float_as_uint(Lwg), tag0, tag1);
    }
  }
  STAMP(5);
  SB();
  REID();
  __builtin_amdgcn_s_setprio(1);
#pragma unroll
  for (int t = 0; t < 8; ++t) vr[t] = LDKV(rv, t);
  SB();
  {
    u32x4 lo, hi;
#pragma unroll
    for (int j = 0; j < 4; ++j) { lo[j] = plo(tw[0][j], tw[1][j]); hi[j] = phi(tw[0][j], tw[1][j]); }
    const int tt = w < 10 ? tid : tid - 640;
    const int Q = tt / 10, ch = tt - Q * 10;
    if (w < 15) {
      const int o = w < 10 ? L_WO + (ch * 64 + Q) * 4 : L_WD + (ch * 32 + Q) * 4;
      *(u32x4*)&lds[o] = lo;
      *(u32x4*)&lds[o + (w < 10 ? 2560 : 1280)] = hi;
    }
#pragma unroll
    for (int j = 0; j < 4; ++j) { lo[j] = plo(gwt[0][j], gwt[1][j]); hi[j] = phi(gwt[0][j], gwt[1][j]); }
    gwt[0] = lo; gwt[1] = hi;
    PIN(gwt[0]); PIN(gwt[1]);
    if (roleG && w < 10) {
      const unsigned ones = 0x3F803F80u;
      float v[8];
#pragma unroll
      for (int j = 0; j < 4; ++j) { v[2 * j] = dot2(lo[j], ones, 0.f); v[2 * j + 1] = dot2(hi[j], ones, 0.f); }
      float s4[4];
#pragma unroll
      for (int i = 0; i < 4; ++i) s4[i] = tr32(v[i], v[i + 4]);
      const f32x2 u = {tr16(s4[0], s4[2]), tr16(s4[1], s4[3])};
      *(f32x2*)&lds[L_CSP + w * 128 + 2 * lane] = u;
      FENCE();
      unsigned old = 0u;
      if (lane == 0) old = __hip_atomic_fetch_add((LAS unsigned*)&ctl[3], 1u, __ATOMIC_RELAXED, __HIP_MEMORY_SCOPE_WORKGROUP);
      if (__builtin_amdgcn_readfirstlane(old) == 9u) {
        FENCE();
        f32x2 t = {0.f, 0.f};
#pragma unroll
        for (int ww = 0; ww < 10; ++ww) t += *(const f32x2*)&lds[L_CSP + ww * 128 + 2 * lane];
        gst(rs, WS_CS + (idx * 8 + xcd) * 1024 + l16, __float_as_uint(t.x), __float_as_uint(t.y), tag0, tag1);
      }
    }
  }
  SB();
  __builtin_amdgcn_s_setprio(0);
#pragma unroll
  for (int t = 8; t < 16; ++t) vr[t] = LDKV(rv, t);
  SB();
  REID();
  const int mloff = w == 0 ? WS_ML + ((b * NH + head) * 4 + (lane & 3)) * 16 : OOB;
  u32x4 mls;
  {
    float a[8];
#pragma unroll
    for (int i = 0; i < 8; ++i) a[i] = 0.f;
#pragma unroll
    for (int u = 0; u < 8; ++u) {
      const u32x4 v0 = vr[2 * u], v1 = vr[2 * u + 1];
      unsigned pp;
      switch (u) {
        case 0: pp = dppu<BC + 0>(ppk); break;
        case 1: pp = dppu<BC + 4>(ppk); break;
        case 2: pp = dppu<BC + 2>(ppk); break;
        case 3: pp = dppu<BC + 6>(ppk); break;
        case 4: pp = dppu<BC + 1>(ppk); break;
        case 5: pp = dppu<BC + 5>(ppk); break;
        case 6: pp = dppu<BC + 3>(ppk); break;
        default: pp = dppu<BC + 7>(ppk); break;
      }
#pragma unroll
      for (int j = 0; j < 4; ++j) {
        a[2 * j] = dot2(plo(v0[j], v1[j]), pp, a[2 * j]);
        a[2 * j + 1] = dot2(phi(v0[j], v1[j]), pp, a[2 * j + 1]);
      }
#pragma unroll
      for (int i = 0; i < 8; ++i) PIN(a[i]);
      SB();
      if (u == 3) mls = gld(rs, mloff, 0);
      SB();
    }
    REID();
    float s4[4];
#pragma unroll
    for (int i = 0; i < 4; ++i) s4[i] = tr32(a[i], a[i + 4]);
    f32x2 u = {tr16(s4[0], s4[2]), tr16(s4[1], s4[3])};
    *(f32x2*)&lds[L_PART + w * 128 + 8 * c + 2 * r] = u;
  }
  STAMP(7);
  float wgt = 0.f;
  if (w == 0) {
    for (unsigned spins = 0; !__all(tagok(mls, tag0, tag1)) && spins < SPIN_CAP; ++spins) {
      FENCE();
      mls = gld(rs, mloff, 0);
    }
    FENCE();
    const float ms = __uint_as_float(mls.x), ls = __uint_as_float(mls.z);
    float M = fmaxf(ms, dppf<X1>(ms)); M = fmaxf(M, dppf<X2>(M));
    float L = ls * __builtin_amdgcn_exp2f(ms - M);
    L += dppf<X1>(L); L += dppf<X2>(L);
    wgt = __builtin_amdgcn_exp2f(lds[L_WML + 2 * c] - M) / L;
  }
  STAMP(6);
  wg_barrier();
  if (w == 0) {
    f32x2 o2 = {0.f, 0.f};
#pragma unroll
    for (int ww = 0; ww < 16; ++ww) o2 += *(const f32x2*)&lds[L_PART + ww * 128 + 2 * lane] * rdl(wgt, ww);
    gst(rs, WS_OX + lid * 1024 + l16, __float_as_uint(o2.x), __float_as_uint(o2.y), tag0, tag1);
  }
  STAMP(8);
  if (w >= 8) {
    const int bb = (w >> 2) & 1, Q = (w & 3) * 16 + (lane >> 2);
    int vo[1] = {WS_OX + (head * 8 + bb * 4 + (lane & 3)) * 1024 + Q * 16};
    f32x2 g[1];
    gather<1>(rs, vo, g, tag0, tag1);
    float o0 = g[0].x, o1 = g[0].y;
    o0 += dppf<X1>(o0); o1 += dppf<X1>(o1);
    o0 += dppf<X2>(o0); o1 += dppf<X2>(o1);
    if ((lane & 3) == 0) ((unsigned*)lds)[L_OPK + Q * 2 + bb] = cvtpk(o0, o1);
  }
  u32x4 wl, wh;
  if (w < 10) { wl = *(const u32x4*)&lds[L_WO + tid * 4]; wh = *(const u32x4*)&lds[L_WO + 2560 + tid * 4]; PIN(wl); PIN(wh); }
  unsigned xr = 0u;
  if (roleG && w < 10) xr = *(const GAS unsigned*)(x_ + ((lane >> 3) & 1) * (D * 2) + xcd * 160 + w * 16 + r * 4);
  wg_barrier();
  STAMP(9);
  if (w < 10) {
    const u32x2 op = *(const u32x2*)&lds[L_OPK + lane * 2];
    float v[16];
#pragma unroll
    for (int j = 0; j < 4; ++j) {
      v[2 * j] = dot2(wl[j], op.x, 0.f); v[2 * j + 1] = dot2(wh[j], op.x, 0.f);
      v[8 + 2 * j] = dot2(wl[j], op.y, 0.f); v[8 + 2 * j + 1] = dot2(wh[j], op.y, 0.f);
    }
    float s8[8], s4[4];
#pragma unroll
    for (int i = 0; i < 8; ++i) s8[i] = tr32(v[i], v[i + 8]);
#pragma unroll
    for (int i = 0; i < 4; ++i) s4[i] = tr16(s8[i], s8[i + 4]);
    float t0 = trd<X8>(s4[0], s4[2], (c & 8) != 0), t1 = trd<X8>(s4[1], s4[3], (c & 8) != 0);
    t0 += dppf<X1>(t0); t1 += dppf<X1>(t1);
    t0 += dppf<X2>(t0); t1 += dppf<X2>(t1);
    t0 += dppf<X7>(t0); t1 += dppf<X7>(t1);
    if ((lane & 7) == 0) gst(rs, WS_Y + ((sub * 10 + w) * 32 + head) * 128 + lane * 2, __float_as_uint(t0), __float_as_uint(t1), tag0, tag1);
  }
  STAMP(10);
  if (roleG) {
    const int gsel = (((lane >> 3) & 1) * 4 + r) * 16;
    float h0 = 0.f, h1 = 0.f;
    if (w >= 8) {
      int vo[1] = {WS_CS + (idx * 8 + (lane & 7)) * 1024 + (w - 8) * 128 + (lane >> 3) * 16};
      f32x2 g[1];
      gather<1>(rs, vo, g, tag0, tag1);
      f32x2 t = g[0];
      t.x += dppf<X1>(t.x); t.y += dppf<X1>(t.y);
      t.x += dppf<X2>(t.x); t.y += dppf<X2>(t.y);
      t.x += dppf<X7>(t.x); t.y += dppf<X7>(t.y);
      if ((lane & 7) == 0) *(f32x2*)&lds[L_CS + ((w - 8) * 8 + (lane >> 3)) * 2] = t;
    }
    if (w < 10) {
      const int vb = WS_Y + ((xcd * 10 + w) * 32 + (lane & 7)) * 128 + gsel;
      int vo[4] = {vb, vb + 1024, vb + 2048, vb + 3072};
      f32x2 g[4];
      gather<4>(rs, vo, g, tag0, tag1);
      STAMP(11);
      float a0 = (g[0].x + g[1].x) + (g[2].x + g[3].x), a1 = (g[0].y + g[1].y) + (g[2].y + g[3].y);
      a0 += dppf<X1>(a0); a1 += dppf<X1>(a1);
      a0 += dppf<X2>(a0); a1 += dppf<X2>(a1);
      a0 += dppf<X7>(a0); a1 += dppf<X7>(a1);
      { const unsigned ab = cvtpk(a0, a1); a0 = bflo(ab); a1 = bfhi(ab); }
      const unsigned pk = cvtpk(bflo(xr) + a0, bfhi(xr) + a1);
      h0 = bflo(pk); h1 = bfhi(pk);
      const unsigned po = dppu<X8>(pk);
      const unsigned hb0 = (lane & 8) ? po : pk, hb1 = (lane & 8) ? pk : po;
      float v[16];
#pragma unroll
      for (int j = 0; j < 4; ++j) {
        v[2 * j] = dot2(gwt[0][j], hb0, 0.f); v[2 * j + 1] = dot2(gwt[1][j], hb0, 0.f);
        v[8 + 2 * j] = dot2(gwt[0][j], hb1, 0.f); v[8 + 2 * j + 1] = dot2(gwt[1][j], hb1, 0.f);
      }
      float st = tr32(h0 + h1, dot2(pk, pk, 0.f));
      float s8[8];
#pragma unroll
      for (int i = 0; i < 8; ++i) s8[i] = tr32(v[i], v[i + 8]);
      st = tr16(st, st);
      f32x4 s4;
#pragma unroll
      for (int i = 0; i < 4; ++i) s4[i] = tr16(s8[i], s8[i + 4]);
      *(f32x4*)&lds[L_PART + tid * 4] = s4;
      if ((lane & 23) == 0) lds[L_ST + w * 4 + ((lane >> 3) & 1) * 2 + (lane >> 5)] = st;
    }
    wg_barrier();
    if (tid < 130) {
      const int o = tid < 128 ? L_PART + tid * 2 : L_ST + (tid - 128) * 2, st = tid < 128 ? 256 : 4;
      f32x2 t = {0.f, 0.f};
#pragma unroll
      for (int ww = 0; ww < 10; ++ww) t += *(const f32x2*)&lds[o + ww * st];
      gst(rs, WS_GP + (idx * 8 + xcd) * GP_REC + tid * 16, __float_as_uint(t.x), __float_as_uint(t.y), tag0, tag1);
    }
    STAMP(12);
    if (w < 10) { wl = *(const u32x4*)&lds[L_WD + (w * 32 + (lane & 31)) * 4]; wh = *(const u32x4*)&lds[L_WD + 1280 + (w * 32 + (lane & 31)) * 4]; PIN(wl); PIN(wh); }
    {
      const int wl2 = (w + 3) & 15;
      const int vb = WS_GP + (idx * 8 + (lane & 7)) * GP_REC + (lane >> 3) * 16;
      const bool st2 = w == 5 && lane < 16;
      int vo[2] = {vb + wl2 * 128, st2 ? vb + 2048 : OOB};
      f32x2 g[2];
      gather<2>(rs, vo, g, tag0, tag1);
      f32x2 t = g[0], t2 = g[1];
      t.x += dppf<X1>(t.x); t.y += dppf<X1>(t.y); t2.x += dppf<X1>(t2.x); t2.y += dppf<X1>(t2.y);
      t.x += dppf<X2>(t.x); t.y += dppf<X2>(t.y); t2.x += dppf<X2>(t2.x); t2.y += dppf<X2>(t2.y);
      t.x += dppf<X7>(t.x); t.y += dppf<X7>(t.y); t2.x += dppf<X7>(t2.x); t2.y += dppf<X7>(t2.y);
      if ((lane & 7) == 0) {
        *(f32x2*)&lds[L_G2 + (wl2 * 8 + (lane >> 3)) * 2] = t;
        if (st2) *(f32x2*)&lds[L_G2 + (128 + (lane >> 3)) * 2] = t2;
      }
    }
    wg_barrier();
    STAMP(13);
    if (w < 10) {
      const int k8 = (lane >> 2) & 7, n2 = (lane >> 1) & 1, n1 = lane & 1, bb = lane >> 5;
      const int tg = bb * 64 + n2 * 32 + k8 * 2 + n1, tc = n2 * 32 + n1 * 16 + k8;
      const f32x2 g = *(const f32x2*)&lds[L_G2 + tg * 2], u = *(const f32x2*)&lds[L_G2 + (tg + 16) * 2];
      const f32x2 cg = *(const f32x2*)&lds[L_CS + tc * 2], cu = *(const f32x2*)&lds[L_CS + (tc + 8) * 2];
      const f32x2 st = *(const f32x2*)&lds[L_G2 + (128 + bb) * 2];
      const float mean = st.x * (1.f / D);
      const float var = fmaxf(fmaf(-mean, mean, st.y * (1.f / D)), 0.f);
      const float mb = bfr(mean), rstd = __builtin_amdgcn_rcpf(bfr(__builtin_amdgcn_sqrtf(bfr(bfr(var) + 1e-5f))));
      const unsigned gb = cvtpk(fmaf(-mb, cg.x, g.x) * rstd, fmaf(-mb, cg.y, g.y) * rstd);
      const unsigned ub = cvtpk(fmaf(-mb, cu.x, u.x) * rstd, fmaf(-mb, cu.y, u.y) * rstd);
      const unsigned hh = cvtpk(bflo(gb) * bflo(ub), bfhi(gb) * bfhi(ub));
      float v[8];
#pragma unroll
      for (int j = 0; j < 4; ++j) { v[2 * j] = dot2(wl[j], hh, 0.f); v[2 * j + 1] = dot2(wh[j], hh, 0.f); }
      float s4[4];
#pragma unroll
      for (int i = 0; i < 4; ++i) s4[i] = tr16(v[i], v[i + 4]);
      float t0 = trd<X8>(s4[0], s4[2], (c & 8) != 0), t1 = trd<X8>(s4[1], s4[3], (c & 8) != 0);
      t0 += dppf<X1>(t0); t1 += dppf<X1>(t1);
      t0 += dppf<X2>(t0); t1 += dppf<X2>(t1);
      t0 += dppf<X7>(t0); t1 += dppf<X7>(t1);
      if ((lane & 7) == 0) gst(rs, WS_OP + ((xcd * 10 + w) * 32 + idx) * 128 + lane * 2, __float_as_uint(t0), __float_as_uint(t1), tag0, tag1);
      STAMP(14);
      if (w == idx) {
        const int vb = WS_OP + ((xcd * 10 + w) * 32 + (lane & 7)) * 128 + gsel;
        int vo[4] = {vb, vb + 1024, vb + 2048, (lane & 7) < 3 ? vb + 3072 : OOB};
        f32x2 g4[4];
        gather<4>(rs, vo, g4, tag0, tag1);
        float a0 = (g4[0].x + g4[1].x) + (g4[2].x + g4[3].x), a1 = (g4[0].y + g4[1].y) + (g4[2].y + g4[3].y);
        a0 += dppf<X1>(a0); a1 += dppf<X1>(a1);
        a0 += dppf<X2>(a0); a1 += dppf<X2>(a1);
        a0 += dppf<X7>(a0); a1 += dppf<X7>(a1);
        { const unsigned ab = cvtpk(a0, a1); a0 = bflo(ab); a1 = bfhi(ab); }
        if ((lane & 7) == 0) *(GAS unsigned*)(out + ((lane >> 3) & 1) * (D * 2) + xcd * 160 + w * 16 + r * 4) = cvtpk(h0 + a0, h1 + a1);
        if (bid == 0 && lane == 0) {
          const u32x2 tg2 = {tag0, tag1};
          __builtin_amdgcn_raw_buffer_store_b64(tg2, rs, WS_EPOCH, 0, SC1);
        }
        STAMP(15);
      }
    }
  }
  STAMP_OUT();
}

extern "C" void kernel_launch(void* const* d_in, const int* in_sizes, int n_in, void* d_out, int out_size, void* d_ws,
                              size_t ws_size, hipStream_t stream) {
  (void)in_sizes; (void)n_in; (void)out_size; (void)ws_size;
  fused_decode<<<NWG, NT, 0, stream>>>((const GAS char*)d_in[0], (const GAS char*)d_in[1], (const GAS char*)d_in[2],
                                       (const GAS char*)d_in[3], (const GAS char*)d_in[4], (const GAS char*)d_in[6],
                                       (const GAS char*)d_in[7], (const GAS char*)d_in[8], (GAS char*)d_out, (char*)d_ws);
}
```

```cpp
#define STAMPS 1
#include <cstdio>
#include <hip/hip_runtime.h>
#include <stdint.h>

#define MIMIC 1

typedef unsigned u32x4 __attribute__((ext_vector_type(4)));
typedef unsigned u32x2 __attribute__((ext_vector_type(2)));
typedef float f32x4 __attribute__((ext_vector_type(4)));
typedef float f32x2 __attribute__((ext_vector_type(2)));
typedef __bf16 bf16x2 __attribute__((ext_vector_type(2)));
#define GAS __attribute__((address_space(1)))
#define LAS __attribute__((address_space(3)))
#define WAUX 2
#define PREF 1
#define ABL 0
#define PRIO 1

constexpr int D = 640, NH = 32, SK = 4096, DH = 128, FF = 1728;
constexpr int NWG = 256, NT = 1024;
constexpr unsigned SPIN_CAP = 1u << 22;

constexpr int WS_EPOCH = 0;
constexpr int WS_QX = 256;
constexpr int WS_ML = WS_QX + 256 * 2048;
constexpr int WS_OX = WS_ML + 4096;
constexpr int WS_Y = WS_OX + 256 * 1024;
constexpr int GP_REC = 17 * 128;
constexpr int WS_GP = WS_Y + 80 * 4096;
constexpr int WS_CS = WS_GP + 216 * GP_REC;
constexpr int WS_OP = WS_CS + 216 * 1024;
constexpr int WS_END = WS_OP + 80 * 4096;
static_assert(WS_OX % 128 == 0 && WS_Y % 128 == 0 && WS_GP % 128 == 0 && WS_OP % 128 == 0, "line alignment");

constexpr int L_PART = 0;
constexpr int L_QPK = L_PART + 2560;
constexpr int L_WML = L_QPK + 64;
constexpr int L_CTL = L_WML + 32;
constexpr int L_OPK = L_CTL + 8;
constexpr int L_ST = L_OPK + 128;
constexpr int L_G2 = L_ST + 40;
constexpr int L_CSP = L_G2 + 272;
constexpr int L_CS = L_CSP + 1280;
constexpr int L_WO = L_CS + 128;
constexpr int L_WD = L_WO + 5120;
constexpr int L_END = L_WD + 2560;

__device__ __forceinline__ float dot2(unsigned a, unsigned b, float c) {
  return __builtin_amdgcn_fdot2_f32_bf16(__builtin_bit_cast(bf16x2, a), __builtin_bit_cast(bf16x2, b), c, false);
}
__device__ __forceinline__ unsigned cvtpk(float lo, float hi) {
  bf16x2 v = {(__bf16)lo, (__bf16)hi};
  return __builtin_bit_cast(unsigned, v);
}
__device__ __forceinline__ float bflo(unsigned u) { return __builtin_bit_cast(float, u << 16); }
__device__ __forceinline__ float bfhi(unsigned u) { return __builtin_bit_cast(float, u & 0xffff0000u); }
__device__ __forceinline__ float bfr(float f) { return bflo(cvtpk(f, f)); }
__device__ __forceinline__ unsigned plo(unsigned a, unsigned b) { return __builtin_amdgcn_perm(b, a, 0x05040100u); }
__device__ __forceinline__ unsigned phi(unsigned a, unsigned b) { return __builtin_amdgcn_perm(b, a, 0x07060302u); }
template <int CTRL> __device__ __forceinline__ float dppf(float x) {
  return __builtin_bit_cast(float, __builtin_amdgcn_mov_dpp(__builtin_bit_cast(int, x), CTRL, 0xf, 0xf, true));
}
template <int CTRL> __device__ __forceinline__ unsigned dppu(unsigned x) {
  return (unsigned)__builtin_amdgcn_mov_dpp((int)x, CTRL, 0xf, 0xf, true);
}
constexpr int X1 = 0xB1, X2 = 0x4E, X7 = 0x141, X8 = 0x128, ROR4 = 0x124, ROR2 = 0x122, ROR1 = 0x121, BC = 0x150;
__device__ __forceinline__ float tr32(float a, float b) {
  auto s = __builtin_amdgcn_permlane32_swap(__float_as_uint(a), __float_as_uint(b), false, false);
  return __uint_as_float(s[0]) + __uint_as_float(s[1]);
}
__device__ __forceinline__ float tr16(float a, float b) {
  auto s = __builtin_amdgcn_permlane16_swap(__float_as_uint(a), __float_as_uint(b), false, false);
  return __uint_as_float(s[0]) + __uint_as_float(s[1]);
}
__device__ __forceinline__ float mx32(float a) {
  auto s = __builtin_amdgcn_permlane32_swap(__float_as_uint(a), __float_as_uint(a), false, false);
  return fmaxf(__uint_as_float(s[0]), __uint_as_float(s[1]));
}
__device__ __forceinline__ float mx16(float a) {
  auto s = __builtin_amdgcn_permlane16_swap(__float_as_uint(a), __float_as_uint(a), false, false);
  return fmaxf(__uint_as_float(s[0]), __uint_as_float(s[1]));
}
template <int CTRL> __device__ __forceinline__ float trd(float a, float b, bool sel) {
  const float t1 = a + dppf<CTRL>(a), t2 = b + dppf<CTRL>(b);
  return sel ? t2 : t1;
}
__device__ __forceinline__ float row_sum(float x) {
  x += dppf<X8>(x); x += dppf<ROR4>(x); x += dppf<X2>(x); x += dppf<X1>(x); return x;
}
__device__ __forceinline__ float row_max(float x) {
  x = fmaxf(x, dppf<X8>(x)); x = fmaxf(x, dppf<ROR4>(x)); x = fmaxf(x, dppf<X2>(x)); x = fmaxf(x, dppf<X1>(x)); return x;
}
__device__ __forceinline__ float wave_sum(float x) { x = row_sum(x); x = tr16(x, x); return tr32(x, x); }
__device__ __forceinline__ float wave_max(float x) { x = row_max(x); x = mx16(x); return mx32(x); }
__device__ __forceinline__ float rdl(float x, int l) { return __int_as_float(__builtin_amdgcn_readlane(__float_as_int(x), l)); }
__device__ __forceinline__ void wg_barrier() { asm volatile("s_waitcnt lgkmcnt(0)\n\ts_barrier" ::: "memory"); }
#define SB() __builtin_amdgcn_sched_barrier(0)
constexpr int NSTAMP = 16, WS_STAMP = 16 << 20;
#define STAMP(i) do { SB(); unsigned long long t_; asm volatile("s_memrealtime %0\n\ts_waitcnt lgkmcnt(0)" : "=s"(t_) :: "memory"); st_[i] = (unsigned)t_ | 1u; SB(); } while (0)
#define STAMP_DECL unsigned st_[NSTAMP]; _Pragma("unroll") for (int i_ = 0; i_ < NSTAMP; ++i_) st_[i_] = 0u
#define STAMP_OUT() do { if ((threadIdx.x & 63) == 0) { _Pragma("unroll") for (int i_ = 0; i_ < NSTAMP; ++i_) ((unsigned*)(ws + WS_STAMP))[(blockIdx.x * 16 + (threadIdx.x >> 6)) * NSTAMP + i_] = st_[i_]; } } while (0)

constexpr int SC1 = 16, NTP = 2;
constexpr int OOB = 0x40000000;
typedef __amdgpu_buffer_rsrc_t rsrc_t;
__device__ __forceinline__ rsrc_t mk_rsrc(const GAS char* p, int bytes) {
  return __builtin_amdgcn_make_buffer_rsrc((void*)p, 0, bytes, 0x00020000);
}
template <int AUX> __device__ __forceinline__ u32x4 bld(rsrc_t r, int voff, int soff) {
  return __builtin_amdgcn_raw_buffer_load_b128(r, voff, soff, AUX);
}
#define PIN(x) asm volatile("" : "+v"(x))

__device__ __forceinline__ bool tagok(u32x4 v, unsigned t0, unsigned t1) { return v.y == t0 && v.w == t1; }
__device__ __forceinline__ u32x4 gld(rsrc_t rs, int voff, int soff) { return __builtin_amdgcn_raw_buffer_load_b128(rs, voff, soff, SC1); }
__device__ __forceinline__ void gst(rsrc_t rs, int voff, unsigned a, unsigned b, unsigned t0, unsigned t1) {
  const u32x4 g = {a, t0, b, t1};
  __builtin_amdgcn_raw_buffer_store_b128(g, rs, voff, 0, SC1);
}
#define FENCE() asm volatile("" ::: "memory")
#define PSLEEP 2
template <int N> __device__ __forceinline__ void gather(rsrc_t rs, int (&vo)[N], f32x2 (&v)[N], unsigned t0, unsigned t1) {
#pragma unroll
  for (int i = 0; i < N; ++i) { v[i].x = 0.f; v[i].y = 0.f; }
  for (unsigned spins = 0;; ++spins) {
    FENCE();
    u32x4 g[N];
#pragma unroll
    for (int i = 0; i < N; ++i) g[i] = gld(rs, vo[i], 0);
    bool more = false;
#pragma unroll
    for (int i = 0; i < N; ++i) {
      const bool ok = tagok(g[i], t0, t1) && vo[i] != OOB;
      v[i].x = ok ? __uint_as_float(g[i].x) : v[i].x;
      v[i].y = ok ? __uint_as_float(g[i].z) : v[i].y;
      vo[i] = ok ? OOB : vo[i];
      more = more || vo[i] != OOB;
    }
    if (!__any(more) || spins > SPIN_CAP) break;
    if (PSLEEP) __builtin_amdgcn_s_sleep(PSLEEP);
  }
  FENCE();
}

__global__ void __launch_bounds__(NT, 4)
fused_decode(const GAS char* __restrict__ x_, const GAS char* __restrict__ kc, const GAS char* __restrict__ vc,
             const GAS char* __restrict__ wq, const GAS char* __restrict__ wo, const GAS char* __restrict__ wg,
             const GAS char* __restrict__ wu, const GAS char* __restrict__ wd, GAS char* __restrict__ out, char* ws) {
  __shared__ __attribute__((aligned(16))) float lds[L_END];
  STAMP_DECL;
  STAMP(0);
  int tid = threadIdx.x, lane = tid & 63;
  const int w = __builtin_amdgcn_readfirstlane(tid >> 6);
  int r = lane >> 4, c = lane & 15;
#define REID() do { asm volatile("" : "+v"(tid)); lane = tid & 63; r = lane >> 4; c = lane & 15; } while (0)
  const int bid = blockIdx.x, xcd = bid & 7, idx = bid >> 3;
  const int head = xcd * 4 + (idx >> 3), b = (idx >> 2) & 1, s = idx & 3;
  const rsrc_t rs = __builtin_amdgcn_make_buffer_rsrc((void*)ws, 0, WS_END, 0x00020000);
  const bool roleG = idx < 27;
  const int sub = idx & 7, lid = head * 8 + sub;
  volatile LAS unsigned* ctl = (volatile LAS unsigned*)&lds[L_CTL];
  const int l16 = lane * 16;

  if (tid < 4) ctl[tid] = 0u;
  __builtin_amdgcn_s_setprio(3);
  wg_barrier();
  if (w < 10) {
    const GAS unsigned* xdw = (const GAS unsigned*)x_;
    const int P = 40 * sub + 4 * w + r;
    const rsrc_t rq = mk_rsrc(wq + head * 256, (ABL & 1) ? 0 : D * 8192 - head * 256);
    unsigned xa[10];
#pragma unroll
    for (int k = 0; k < 10; ++k) xa[k] = xdw[(lane >> 5) * 320 + (lane & 31) + 32 * k];
    const unsigned xb0 = xdw[P], xb1 = xdw[320 + P];
    const u32x4 wq0 = bld<WAUX>(rq, (2 * P) * 8192 + c * 16, 0);
    const u32x4 wq1 = bld<WAUX>(rq, (2 * P) * 8192 + c * 16, 8192);
    const u32x2 ep = __builtin_amdgcn_raw_buffer_load_b64(rs, w == 0 ? WS_EPOCH : OOB, 0, SC1);
    SB();
    STAMP(1);
    const unsigned ones = 0x3F803F80u;
    float sm = 0.f;
#pragma unroll
    for (int k = 0; k < 10; ++k) sm = dot2(xa[k], ones, sm);
    sm = row_sum(sm); sm = tr16(sm, sm);
    const float mean = sm * (1.f / D);
    float sq = 0.f, sq2 = 0.f;
#pragma unroll
    for (int k = 0; k < 10; ++k) { const float a = bflo(xa[k]) - mean, bb = bfhi(xa[k]) - mean; sq = fmaf(a, a, sq); sq2 = fmaf(bb, bb, sq2); }
    sq += sq2;
    sq = row_sum(sq); sq = tr16(sq, sq);
    const float var = sq * (1.f / D);
    const float mb = bfr(mean), rstd = __builtin_amdgcn_rcpf(bfr(__builtin_amdgcn_sqrtf(bfr(bfr(var) + 1e-5f))));
    const float m0 = rdl(mb, 0), m1 = rdl(mb, 32), d0 = rdl(rstd, 0), d1 = rdl(rstd, 32);
    const unsigned xn0 = cvtpk(bfr(bflo(xb0) - m0) * d0, bfr(bfhi(xb0) - m0) * d0);
    const unsigned xn1 = cvtpk(bfr(bflo(xb1) - m1) * d1, bfr(bfhi(xb1) - m1) * d1);
    float a0[8], a1[8];
#pragma unroll
    for (int j = 0; j < 4; ++j) {
      const unsigned lo = plo(wq0[j], wq1[j]), hi = phi(wq0[j], wq1[j]);
      a0[2 * j] = dot2(lo, xn0, 0.f); a0[2 * j + 1] = dot2(hi, xn0, 0.f);
      a1[2 * j] = dot2(lo, xn1, 0.f); a1[2 * j + 1] = dot2(hi, xn1, 0.f);
    }
    float s0[4], s1[4];
#pragma unroll
    for (int i = 0; i < 4; ++i) { s0[i] = tr32(a0[i], a0[i + 4]); s1[i] = tr32(a1[i], a1[i + 4]); }
    f32x2 u0 = {tr16(s0[0], s0[2]), tr16(s0[1], s0[3])}, u1 = {tr16(s1[0], s1[2]), tr16(s1[1], s1[3])};
    *(f32x2*)&lds[L_PART + w * 256 + 8 * c + 2 * r] = u0;
    *(f32x2*)&lds[L_PART + w * 256 + 128 + 8 * c + 2 * r] = u1;
    FENCE();
    if (lane == 0) __hip_atomic_fetch_add((LAS unsigned*)&ctl[0], 1u, __ATOMIC_RELAXED, __HIP_MEMORY_SCOPE_WORKGROUP);
    STAMP(2);
    if (w == 0) {
      const unsigned long long e64 = ((unsigned long long)ep.y << 32) | ep.x;
      const unsigned long long tg = e64 * 6364136223846793005ull + 1442695040888963407ull;
      const unsigned t0 = (unsigned)tg, t1 = (unsigned)(tg >> 32);
      if (lane == 0) { ctl[4] = t0; ctl[5] = t1; }
      while (ctl[0] < 10u) {}
      FENCE();
      f32x2 p0 = {0.f, 0.f}, p1 = {0.f, 0.f};
#pragma unroll
      for (int ww = 0; ww < 10; ++ww) {
        p0 += *(const f32x2*)&lds[L_PART + ww * 256 + 2 * lane];
        p1 += *(const f32x2*)&lds[L_PART + ww * 256 + 128 + 2 * lane];
      }
      gst(rs, WS_QX + lid * 2048 + l16, __float_as_uint(p0.x), __float_as_uint(p0.y), t0, t1);
      gst(rs, WS_QX + lid * 2048 + 1024 + l16, __float_as_uint(p1.x), __float_as_uint(p1.y), t0, t1);
      STAMP(3);
      f32x2 qs;
      for (unsigned spins = 0;; ++spins) {
        FENCE();
        u32x4 qq[8];
#pragma unroll
        for (int k = 0; k < 8; ++k) qq[k] = gld(rs, WS_QX + head * 16384 + b * 1024 + l16, k * 2048);
        bool ok = true;
        qs.x = 0.f; qs.y = 0.f;
#pragma unroll
        for (int k = 0; k < 8; ++k) {
          ok = ok && tagok(qq[k], t0, t1);
          qs.x += __uint_as_float(qq[k].x); qs.y += __uint_as_float(qq[k].z);
        }
        if (__all(ok) || spins > SPIN_CAP) break;
      }
      ((unsigned*)lds)[L_QPK + lane] = cvtpk(qs.x, qs.y);
      FENCE();
      if (lane == 0) ctl[1] = 1u;
    }
  }
  SB();
  REID();
  __builtin_amdgcn_s_setprio(2);
  const size_t kvoff = ((size_t)((b * NH + head) * SK + s * 1024 + w * 64)) * 256;
  const rsrc_t rk = mk_rsrc(kc + kvoff, 16384), rv = mk_rsrc(vc + kvoff, 16384);
#define LDKV(R, t) bld<NTP>(R, l16, (t) * 1024)
  u32x4 kr[16], vr[16], tw[2], gwt[2];
  {
    const int tt = w < 10 ? tid : tid - 640;
    const int Q = tt / 10, ch = tt - Q * 10;
    const bool isd = w >= 10;
    const rsrc_t rt = mk_rsrc(isd ? wd + (size_t)idx * (64 * 1280) + xcd * 160 : wo + (size_t)head * (128 * 1280) + sub * 160,
                              ((ABL & 4) || w == 15 || (isd && !roleG)) ? 0 : 128 * 1280 - 1120);
    const int vo = (2 * Q) * 1280 + ch * 16;
    const GAS char* gp = ((lane & 8) ? wu : wg) + (size_t)(80 * xcd + 8 * w + 2 * r) * (FF * 2) + idx * 128 + (lane & 7) * 16;
    const bool gact = roleG && w < 10 && !(ABL & 2);
    if (!gact) gp = wg;
#pragma unroll
    for (int t = 0; t < 16; ++t) {
      kr[t] = LDKV(rk, t);
      if (t == 2) tw[0] = bld<WAUX>(rt, vo, 0);
      if (t == 5) gwt[0] = __builtin_nontemporal_load((const GAS u32x4*)gp);
      if (t == 8) tw[1] = bld<WAUX>(rt, vo, 1280);
      if (t == 11) gwt[1] = __builtin_nontemporal_load((const GAS u32x4*)(gp + FF * 2));
      SB();
    }
  }
  while (ctl[1] == 0u) __builtin_amdgcn_s_sleep(1);
  FENCE();
  REID();
  const u32x4 qv = *(const u32x4*)&lds[L_QPK + 4 * c];
  const unsigned tag0 = __builtin_amdgcn_readfirstlane(ctl[4]), tag1 = __builtin_amdgcn_readfirstlane(ctl[5]);
  STAMP(4);

  float sc;
  {
    float l1[2], l2[2], l3[2], l4[2];
#pragma unroll
    for (int t = 0; t < 16; ++t) {
      const u32x4 kk = kr[t];
      float d = dot2(kk[0], qv[0], 0.f);
      d = dot2(kk[1], qv[1], d); d = dot2(kk[2], qv[2], d); d = dot2(kk[3], qv[3], d);
      l1[t & 1] = d;
      if ((t & 1) == 1) l2[(t >> 1) & 1] = trd<X8>(l1[0], l1[1], (c & 8) != 0);
      if ((t & 3) == 3) l3[(t >> 2) & 1] = trd<X7>(l2[0], l2[1], (c & 4) != 0);
      if ((t & 7) == 7) l4[(t >> 3) & 1] = trd<X2>(l3[0], l3[1], (c & 2) != 0);
      SB();
    }
    sc = trd<X1>(l4[0], l4[1], (c & 1) != 0);
  }
  const float s2 = bfr(bfr(sc) * 0.08838834764831845f) * 1.4426950408889634f;
  unsigned ppk;
  {
    const float mw = wave_max(s2);
    const float p = __builtin_amdgcn_exp2f(s2 - mw);
    ppk = cvtpk(p, dppf<X8>(p));
    const float lw = wave_sum(p);
    unsigned old = 0u;
    if (lane == 0) {
      f32x2 t = {mw, lw}; *(f32x2*)&lds[L_WML + 2 * w] = t;
      FENCE();
      old = __hip_atomic_fetch_add((LAS unsigned*)&ctl[2], 1u, __ATOMIC_RELAXED, __HIP_MEMORY_SCOPE_WORKGROUP);
    }
    if (__builtin_amdgcn_readfirstlane(old) == 15u) {
      FENCE();
      const f32x2 ml = *(const f32x2*)&lds[L_WML + 2 * c];
      const float Mwg = row_max(ml.x);
      const float Lwg = row_sum(ml.y * __builtin_amdgcn_exp2f(ml.x - Mwg));
      if (lane == 0) gst(rs, WS_ML + ((b * NH + head) * 4 + s) * 16, __float_as_uint(Mwg), __float_as_uint(Lwg), tag0, tag1);
    }
  }
  STAMP(5);
  SB();
  REID();
  __builtin_amdgcn_s_setprio(1);
#pragma unroll
  for (int t = 0; t < 8; ++t) vr[t] = LDKV(rv, t);
  SB();
  {
    u32x4 lo, hi;
#pragma unroll
    for (int j = 0; j < 4; ++j) { lo[j] = plo(tw[0][j], tw[1][j]); hi[j] = phi(tw[0][j], tw[1][j]); }
    const int tt = w < 10 ? tid : tid - 640;
    const int Q = tt / 10, ch = tt - Q * 10;
    if (w < 15) {
      const int o = w < 10 ? L_WO + (ch * 64 + Q) * 4 : L_WD + (ch * 32 + Q) * 4;
      *(u32x4*)&lds[o] = lo;
      *(u32x4*)&lds[o + (w < 10 ? 2560 : 1280)] = hi;
    }
#pragma unroll
    for (int j = 0; j < 4; ++j) { lo[j] = plo(gwt[0][j], gwt[1][j]); hi[j] = phi(gwt[0][j], gwt[1][j]); }
    gwt[0] = lo; gwt[1] = hi;
    PIN(gwt[0]); PIN(gwt[1]);
    if (roleG && w < 10) {
      const unsigned ones = 0x3F803F80u;
      float v[8];
#pragma unroll
      for (int j = 0; j < 4; ++j) { v[2 * j] = dot2(lo[j], ones, 0.f); v[2 * j + 1] = dot2(hi[j], ones, 0.f); }
      float s4[4];
#pragma unroll
      for (int i = 0; i < 4; ++i) s4[i] = tr32(v[i], v[i + 4]);
      const f32x2 u = {tr16(s4[0], s4[2]), tr16(s4[1], s4[3])};
      *(f32x2*)&lds[L_CSP + w * 128 + 2 * lane] = u;
      FENCE();
      unsigned old = 0u;
      if (lane == 0) old = __hip_atomic_fetch_add((LAS unsigned*)&ctl[3], 1u, __ATOMIC_RELAXED, __HIP_MEMORY_SCOPE_WORKGROUP);
      if (__builtin_amdgcn_readfirstlane(old) == 9u) {
        FENCE();
        f32x2 t = {0.f, 0.f};
#pragma unroll
        for (int ww = 0; ww < 10; ++ww) t += *(const f32x2*)&lds[L_CSP + ww * 128 + 2 * lane];
        gst(rs, WS_CS + (idx * 8 + xcd) * 1024 + l16, __float_as_uint(t.x), __float_as_uint(t.y), tag0, tag1);
      }
    }
  }
  SB();
  __builtin_amdgcn_s_setprio(0);
#pragma unroll
  for (int t = 8; t < 16; ++t) vr[t] = LDKV(rv, t);
  SB();
  REID();
  const int mloff = w == 0 ? WS_ML + ((b * NH + head) * 4 + (lane & 3)) * 16 : OOB;
  u32x4 mls;
  {
    float a[8];
#pragma unroll
    for (int i = 0; i < 8; ++i) a[i] = 0.f;
#pragma unroll
    for (int u = 0; u < 8; ++u) {
      const u32x4 v0 = vr[2 * u], v1 = vr[2 * u + 1];
      unsigned pp;
      switch (u) {
        case 0: pp = dppu<BC + 0>(ppk); break;
        case 1: pp = dppu<BC + 4>(ppk); break;
        case 2: pp = dppu<BC + 2>(ppk); break;
        case 3: pp = dppu<BC + 6>(ppk); break;
        case 4: pp = dppu<BC + 1>(ppk); break;
        case 5: pp = dppu<BC + 5>(ppk); break;
        case 6: pp = dppu<BC + 3>(ppk); break;
        default: pp = dppu<BC + 7>(ppk); break;
      }
#pragma unroll
      for (int j = 0; j < 4; ++j) {
        a[2 * j] = dot2(plo(v0[j], v1[j]), pp, a[2 * j]);
        a[2 * j + 1] = dot2(phi(v0[j], v1[j]), pp, a[2 * j + 1]);
      }
#pragma unroll
      for (int i = 0; i < 8; ++i) PIN(a[i]);
      SB();
      if (u == 3) mls = gld(rs, mloff, 0);
      SB();
    }
    REID();
    float s4[4];
#pragma unroll
    for (int i = 0; i < 4; ++i) s4[i] = tr32(a[i], a[i + 4]);
    f32x2 u = {tr16(s4[0], s4[2]), tr16(s4[1], s4[3])};
    *(f32x2*)&lds[L_PART + w * 128 + 8 * c + 2 * r] = u;
  }
  STAMP(7);
  float wgt = 0.f;
  if (w == 0) {
    for (unsigned spins = 0; !__all(tagok(mls, tag0, tag1)) && spins < SPIN_CAP; ++spins) {
      FENCE();
      mls = gld(rs, mloff, 0);
    }
    FENCE();
    const float ms = __uint_as_float(mls.x), ls = __uint_as_float(mls.z);
    float M = fmaxf(ms, dppf<X1>(ms)); M = fmaxf(M, dppf<X2>(M));
    float L = ls * __builtin_amdgcn_exp2f(ms - M);
    L += dppf<X1>(L); L += dppf<X2>(L);
    wgt = __builtin_amdgcn_exp2f(lds[L_WML + 2 * c] - M) / L;
  }
  STAMP(6);
  wg_barrier();
  if (w == 0) {
    f32x2 o2 = {0.f, 0.f};
#pragma unroll
    for (int ww = 0; ww < 16; ++ww) o2 += *(const f32x2*)&lds[L_PART + ww * 128 + 2 * lane] * rdl(wgt, ww);
    gst(rs, WS_OX + lid * 1024 + l16, __float_as_uint(o2.x), __float_as_uint(o2.y), tag0, tag1);
  }
  STAMP(8);
  if (w >= 8) {
    const int bb = (w >> 2) & 1, Q = (w & 3) * 16 + (lane >> 2);
    int vo[1] = {WS_OX + (head * 8 + bb * 4 + (lane & 3)) * 1024 + Q * 16};
    f32x2 g[1];
    gather<1>(rs, vo, g, tag0, tag1);
    float o0 = g[0].x, o1 = g[0].y;
    o0 += dppf<X1>(o0); o1 += dppf<X1>(o1);
    o0 += dppf<X2>(o0); o1 += dppf<X2>(o1);
    if ((lane & 3) == 0) ((unsigned*)lds)[L_OPK + Q * 2 + bb] = cvtpk(o0, o1);
  }
  u32x4 wl, wh;
  if (w < 10) { wl = *(const u32x4*)&lds[L_WO + tid * 4]; wh = *(const u32x4*)&lds[L_WO + 2560 + tid * 4]; PIN(wl); PIN(wh); }
  unsigned xr = 0u;
  if (roleG && w < 10) xr = *(const GAS unsigned*)(x_ + ((lane >> 3) & 1) * (D * 2) + xcd * 160 + w * 16 + r * 4);
  wg_barrier();
  STAMP(9);
  if (w < 10) {
    const u32x2 op = *(const u32x2*)&lds[L_OPK + lane * 2];
    float v[16];
#pragma unroll
    for (int j = 0; j < 4; ++j) {
      v[2 * j] = dot2(wl[j], op.x, 0.f); v[2 * j + 1] = dot2(wh[j], op.x, 0.f);
      v[8 + 2 * j] = dot2(wl[j], op.y, 0.f); v[8 + 2 * j + 1] = dot2(wh[j], op.y, 0.f);
    }
    float s8[8], s4[4];
#pragma unroll
    for (int i = 0; i < 8; ++i) s8[i] = tr32(v[i], v[i + 8]);
#pragma unroll
    for (int i = 0; i < 4; ++i) s4[i] = tr16(s8[i], s8[i + 4]);
    float t0 = trd<X8>(s4[0], s4[2], (c & 8) != 0), t1 = trd<X8>(s4[1], s4[3], (c & 8) != 0);
    t0 += dppf<X1>(t0); t1 += dppf<X1>(t1);
    t0 += dppf<X2>(t0); t1 += dppf<X2>(t1);
    t0 += dppf<X7>(t0); t1 += dppf<X7>(t1);
    if ((lane & 7) == 0) gst(rs, WS_Y + ((sub * 10 + w) * 32 + head) * 128 + lane * 2, __float_as_uint(t0), __float_as_uint(t1), tag0, tag1);
  }
  STAMP(10);
  if (roleG) {
    const int gsel = (((lane >> 3) & 1) * 4 + r) * 16;
    float h0 = 0.f, h1 = 0.f;
    if (w >= 8) {
      int vo[1] = {WS_CS + (idx * 8 + (lane & 7)) * 1024 + (w - 8) * 128 + (lane >> 3) * 16};
      f32x2 g[1];
      gather<1>(rs, vo, g, tag0, tag1);
      f32x2 t = g[0];
      t.x += dppf<X1>(t.x); t.y += dppf<X1>(t.y);
      t.x += dppf<X2>(t.x); t.y += dppf<X2>(t.y);
      t.x += dppf<X7>(t.x); t.y += dppf<X7>(t.y);
      if ((lane & 7) == 0) *(f32x2*)&lds[L_CS + ((w - 8) * 8 + (lane >> 3)) * 2] = t;
    }
    if (w < 10) {
      const int vb = WS_Y + ((xcd * 10 + w) * 32 + (lane & 7)) * 128 + gsel;
      int vo[4] = {vb, vb + 1024, vb + 2048, vb + 3072};
      f32x2 g[4];
      gather<4>(rs, vo, g, tag0, tag1);
      STAMP(11);
      float a0 = (g[0].x + g[1].x) + (g[2].x + g[3].x), a1 = (g[0].y + g[1].y) + (g[2].y + g[3].y);
      a0 += dppf<X1>(a0); a1 += dppf<X1>(a1);
      a0 += dppf<X2>(a0); a1 += dppf<X2>(a1);
      a0 += dppf<X7>(a0); a1 += dppf<X7>(a1);
      { const unsigned ab = cvtpk(a0, a1); a0 = bflo(ab); a1 = bfhi(ab); }
      const unsigned pk = cvtpk(bflo(xr) + a0, bfhi(xr) + a1);
      h0 = bflo(pk); h1 = bfhi(pk);
      const unsigned po = dppu<X8>(pk);
      const unsigned hb0 = (lane & 8) ? po : pk, hb1 = (lane & 8) ? pk : po;
      float v[16];
#pragma unroll
      for (int j = 0; j < 4; ++j) {
        v[2 * j] = dot2(gwt[0][j], hb0, 0.f); v[2 * j + 1] = dot2(gwt[1][j], hb0, 0.f);
        v[8 + 2 * j] = dot2(gwt[0][j], hb1, 0.f); v[8 + 2 * j + 1] = dot2(gwt[1][j], hb1, 0.f);
      }
      float st = tr32(h0 + h1, dot2(pk, pk, 0.f));
      float s8[8];
#pragma unroll
      for (int i = 0; i < 8; ++i) s8[i] = tr32(v[i], v[i + 8]);
      st = tr16(st, st);
      f32x4 s4;
#pragma unroll
      for (int i = 0; i < 4; ++i) s4[i] = tr16(s8[i], s8[i + 4]);
      *(f32x4*)&lds[L_PART + tid * 4] = s4;
      if ((lane & 23) == 0) lds[L_ST + w * 4 + ((lane >> 3) & 1) * 2 + (lane >> 5)] = st;
    }
    wg_barrier();
    if (tid < 130) {
      const int o = tid < 128 ? L_PART + tid * 2 : L_ST + (tid - 128) * 2, st = tid < 128 ? 256 : 4;
      f32x2 t = {0.f, 0.f};
#pragma unroll
      for (int ww = 0; ww < 10; ++ww) t += *(const f32x2*)&lds[o + ww * st];
      gst(rs, WS_GP + (idx * 8 + xcd) * GP_REC + tid * 16, __float_as_uint(t.x), __float_as_uint(t.y), tag0, tag1);
    }
    STAMP(12);
    if (w < 10) { wl = *(const u32x4*)&lds[L_WD + (w * 32 + (lane & 31)) * 4]; wh = *(const u32x4*)&lds[L_WD + 1280 + (w * 32 + (lane & 31)) * 4]; PIN(wl); PIN(wh); }
    {
      const int wl2 = (w + 3) & 15;
      const int vb = WS_GP + (idx * 8 + (lane & 7)) * GP_REC + (lane >> 3) * 16;
      const bool st2 = w == 5 && lane < 16;
      int vo[2] = {vb + wl2 * 128, st2 ? vb + 2048 : OOB};
      f32x2 g[2];
      gather<2>(rs, vo, g, tag0, tag1);
      f32x2 t = g[0], t2 = g[1];
      t.x += dppf<X1>(t.x); t.y += dppf<X1>(t.y); t2.x += dppf<X1>(t2.x); t2.y += dppf<X1>(t2.y);
      t.x += dppf<X2>(t.x); t.y += dppf<X2>(t.y); t2.x += dppf<X2>(t2.x); t2.y += dppf<X2>(t2.y);
      t.x += dppf<X7>(t.x); t.y += dppf<X7>(t.y); t2.x += dppf<X7>(t2.x); t2.y += dppf<X7>(t2.y);
      if ((lane & 7) == 0) {
        *(f32x2*)&lds[L_G2 + (wl2 * 8 + (lane >> 3)) * 2] = t;
        if (st2) *(f32x2*)&lds[L_G2 + (128 + (lane >> 3)) * 2] = t2;
      }
    }
    wg_barrier();
    STAMP(13);
    if (w < 10) {
      const int k8 = (lane >> 2) & 7, n2 = (lane >> 1) & 1, n1 = lane & 1, bb = lane >> 5;
      const int tg = bb * 64 + n2 * 32 + k8 * 2 + n1, tc = n2 * 32 + n1 * 16 + k8;
      const f32x2 g = *(const f32x2*)&lds[L_G2 + tg * 2], u = *(const f32x2*)&lds[L_G2 + (tg + 16) * 2];
      const f32x2 cg = *(const f32x2*)&lds[L_CS + tc * 2], cu = *(const f32x2*)&lds[L_CS + (tc + 8) * 2];
      const f32x2 st = *(const f32x2*)&lds[L_G2 + (128 + bb) * 2];
      const float mean = st.x * (1.f / D);
      const float var = fmaxf(fmaf(-mean, mean, st.y * (1.f / D)), 0.f);
      const float mb = bfr(mean), rstd = __builtin_amdgcn_rcpf(bfr(__builtin_amdgcn_sqrtf(bfr(bfr(var) + 1e-5f))));
      const unsigned gb = cvtpk(fmaf(-mb, cg.x, g.x) * rstd, fmaf(-mb, cg.y, g.y) * rstd);
      const unsigned ub = cvtpk(fmaf(-mb, cu.x, u.x) * rstd, fmaf(-mb, cu.y, u.y) * rstd);
      const unsigned hh = cvtpk(bflo(gb) * bflo(ub), bfhi(gb) * bfhi(ub));
      float v[8];
#pragma unroll
      for (int j = 0; j < 4; ++j) { v[2 * j] = dot2(wl[j], hh, 0.f); v[2 * j + 1] = dot2(wh[j], hh, 0.f); }
      float s4[4];
#pragma unroll
      for (int i = 0; i < 4; ++i) s4[i] = tr16(v[i], v[i + 4]);
      float t0 = trd<X8>(s4[0], s4[2], (c & 8) != 0), t1 = trd<X8>(s4[1], s4[3], (c & 8) != 0);
      t0 += dppf<X1>(t0); t1 += dppf<X1>(t1);
      t0 += dppf<X2>(t0); t1 += dppf<X2>(t1);
      t0 += dppf<X7>(t0); t1 += dppf<X7>(t1);
      if ((lane & 7) == 0) gst(rs, WS_OP + ((xcd * 10 + w) * 32 + idx) * 128 + lane * 2, __float_as_uint(t0), __float_as_uint(t1), tag0, tag1);
      STAMP(14);
      if (w == idx) {
        const int vb = WS_OP + ((xcd * 10 + w) * 32 + (lane & 7)) * 128 + gsel;
        int vo[4] = {vb, vb + 1024, vb + 2048, (lane & 7) < 3 ? vb + 3072 : OOB};
        f32x2 g4[4];
        gather<4>(rs, vo, g4, tag0, tag1);
        float a0 = (g4[0].x + g4[1].x) + (g4[2].x + g4[3].x), a1 = (g4[0].y + g4[1].y) + (g4[2].y + g4[3].y);
        a0 += dppf<X1>(a0); a1 += dppf<X1>(a1);
        a0 += dppf<X2>(a0); a1 += dppf<X2>(a1);
        a0 += dppf<X7>(a0); a1 += dppf<X7>(a1);
        { const unsigned ab = cvtpk(a0, a1); a0 = bflo(ab); a1 = bfhi(ab); }
        if ((lane & 7) == 0) *(GAS unsigned*)(out + ((lane >> 3) & 1) * (D * 2) + xcd * 160 + w * 16 + r * 4) = cvtpk(h0 + a0, h1 + a1);
        if (bid == 0 && lane == 0) {
          const u32x2 tg2 = {tag0, tag1};
          __builtin_amdgcn_raw_buffer_store_b64(tg2, rs, WS_EPOCH, 0, SC1);
        }
        STAMP(15);
      }
    }
  }
  STAMP_OUT();
}

__global__ void print_stamps(const unsigned* st, const void* a, const void* b, const void* c) {
  if (threadIdx.x != 0) return;
  unsigned t0 = 0xffffffffu;
  for (int g = 0; g < 4096; ++g) t0 = min(t0, st[g * NSTAMP]);
  printf("ptrs k=%p v=%p ws=%p\n", a, b, c);
  for (int k = 0; k < NSTAMP; ++k) {
    unsigned mn = 0xffffffffu, mx = 0; unsigned long long sum = 0; int n = 0;
    for (int g = 0; g < 4096; ++g) { unsigned t = st[g * NSTAMP + k]; if (!t) continue; t -= t0; mn = min(mn, t); mx = max(mx, t); sum += t; ++n; }
    if (n) printf("stamp %d n=%d min %u avg %u max %u (x10ns)\n", k, n, mn, (unsigned)(sum / n), mx);
  }
}
extern "C" void kernel_launch(void* const* d_in, const int* in_sizes, int n_in, void* d_out, int out_size, void* d_ws,
                              size_t ws_size, hipStream_t stream) {
  (void)in_sizes; (void)n_in; (void)out_size; (void)ws_size;
  fused_decode<<<NWG, NT, 0, stream>>>((const GAS char*)d_in[0], (const GAS char*)d_in[1], (const GAS char*)d_in[2],
                                       (const GAS char*)d_in[3], (const GAS char*)d_in[4], (const GAS char*)d_in[6],
                                       (const GAS char*)d_in[7], (const GAS char*)d_in[8], (GAS char*)d_out, (char*)d_ws);
  print_stamps<<<1, 64, 0, stream>>>((const unsigned*)((char*)d_ws + WS_STAMP), d_in[1], d_in[2], d_ws);
}
```

```cpp
#include <hip/hip_runtime.h>
#include <stdint.h>

#define MIMIC 1

typedef unsigned u32x4 __attribute__((ext_vector_type(4)));
typedef unsigned u32x2 __attribute__((ext_vector_type(2)));
typedef float f32x4 __attribute__((ext_vector_type(4)));
typedef float f32x2 __attribute__((ext_vector_type(2)));
typedef __bf16 bf16x2 __attribute__((ext_vector_type(2)));
#define GAS __attribute__((address_space(1)))
#define LAS __attribute__((address_space(3)))
#define WAUX 2
#define PREF 1
#define ABL 0
#define PRIO 1

constexpr int D = 640, NH = 32, SK = 4096, DH = 128, FF = 1728;
constexpr int NWG = 256, NT = 1024;
constexpr unsigned SPIN_CAP = 1u << 22;

constexpr int WS_EPOCH = 0;
constexpr int WS_QX = 256;
constexpr int WS_ML = WS_QX + 256 * 2048;
constexpr int WS_OX = WS_ML + 4096;
constexpr int WS_Y = WS_OX + 256 * 1024;
constexpr int GP_REC = 17 * 128;
constexpr int WS_GP = WS_Y + 80 * 4096;
constexpr int WS_CS = WS_GP + 216 * GP_REC;
constexpr int WS_OP = WS_CS + 216 * 1024;
constexpr int WS_END = WS_OP + 80 * 4096;
static_assert(WS_OX % 128 == 0 && WS_Y % 128 == 0 && WS_GP % 128 == 0 && WS_OP % 128 == 0, "line alignment");

constexpr int L_PART = 0;
constexpr int L_QPK = L_PART + 2560;
constexpr int L_WML = L_QPK + 64;
constexpr int L_CTL = L_WML + 32;
constexpr int L_OPK = L_CTL + 8;
constexpr int L_ST = L_OPK + 128;
constexpr int L_G2 = L_ST + 40;
constexpr int L_CSP = L_G2 + 272;
constexpr int L_CS = L_CSP + 1280;
constexpr int L_WO = L_CS + 128;
constexpr int L_WD = L_WO + 5120;
constexpr int L_END = L_WD + 2560;

__device__ __forceinline__ float dot2(unsigned a, unsigned b, float c) {
  return __builtin_amdgcn_fdot2_f32_bf16(__builtin_bit_cast(bf16x2, a), __builtin_bit_cast(bf16x2, b), c, false);
}
__device__ __forceinline__ unsigned cvtpk(float lo, float hi) {
  bf16x2 v = {(__bf16)lo, (__bf16)hi};
  return __builtin_bit_cast(unsigned, v);
}
__device__ __forceinline__ float bflo(unsigned u) { return __builtin_bit_cast(float, u << 16); }
__device__ __forceinline__ float bfhi(unsigned u) { return __builtin_bit_cast(float, u & 0xffff0000u); }
__device__ __forceinline__ float bfr(float f) { return bflo(cvtpk(f, f)); }
__device__ __forceinline__ unsigned plo(unsigned a, unsigned b) { return __builtin_amdgcn_perm(b, a, 0x05040100u); }
__device__ __forceinline__ unsigned phi(unsigned a, unsigned b) { return __builtin_amdgcn_perm(b, a, 0x07060302u); }
template <int CTRL> __device__ __forceinline__ float dppf(float x) {
  return __builtin_bit_cast(float, __builtin_amdgcn_mov_dpp(__builtin_bit_cast(int, x), CTRL, 0xf, 0xf, true));
}
template <int CTRL> __device__ __forceinline__ unsigned dppu(unsigned x) {
  return (unsigned)__builtin_amdgcn_mov_dpp((int)x, CTRL, 0xf, 0xf, true);
}
constexpr int X1 = 0xB1, X2 = 0x4E, X7 = 0x141, X8 = 0x128, ROR4 = 0x124, ROR2 = 0x122, ROR1 = 0x121, BC = 0x150;
__device__ __forceinline__ float tr32(float a, float b) {
  auto s = __builtin_amdgcn_permlane32_swap(__float_as_uint(a), __float_as_uint(b), false, false);
  return __uint_as_float(s[0]) + __uint_as_float(s[1]);
}
__device__ __forceinline__ float tr16(float a, float b) {
  auto s = __builtin_amdgcn_permlane16_swap(__float_as_uint(a), __float_as_uint(b), false, false);
  return __uint_as_float(s[0]) + __uint_as_float(s[1]);
}
__device__ __forceinline__ float mx32(float a) {
  auto s = __builtin_amdgcn_permlane32_swap(__float_as_uint(a), __float_as_uint(a), false, false);
  return fmaxf(__uint_as_float(s[0]), __uint_as_float(s[1]));
}
__device__ __forceinline__ float mx16(float a) {
  auto s = __builtin_amdgcn_permlane16_swap(__float_as_uint(a), __float_as_uint(a), false, false);
  return fmaxf(__uint_as_float(s[0]), __uint_as_float(s[1]));
}
template <int CTRL> __device__ __forceinline__ float trd(float a, float b, bool sel) {
  const float t1 = a + dppf<CTRL>(a), t2 = b + dppf<CTRL>(b);
  return sel ? t2 : t1;
}
__device__ __forceinline__ float row_sum(float x) {
  x += dppf<X8>(x); x += dppf<ROR4>(x); x += dppf<X2>(x); x += dppf<X1>(x); return x;
}
__device__ __forceinline__ float row_max(float x) {
  x = fmaxf(x, dppf<X8>(x)); x = fmaxf(x, dppf<ROR4>(x)); x = fmaxf(x, dppf<X2>(x)); x = fmaxf(x, dppf<X1>(x)); return x;
}
__device__ __forceinline__ float wave_sum(float x) { x = row_sum(x); x = tr16(x, x); return tr32(x, x); }
__device__ __forceinline__ float wave_max(float x) { x = row_max(x); x = mx16(x); return mx32(x); }
__device__ __forceinline__ float rdl(float x, int l) { return __int_as_float(__builtin_amdgcn_readlane(__float_as_int(x), l)); }
__device__ __forceinline__ void wg_barrier() { asm volatile("s_waitcnt lgkmcnt(0)\n\ts_barrier" ::: "memory"); }
#define SB() __builtin_amdgcn_sched_barrier(0)
#define STAMP(i)
#define STAMP_DECL
#define STAMP_OUT()

constexpr int SC1 = 16, NTP = 2;
constexpr int OOB = 0x40000000;
typedef __amdgpu_buffer_rsrc_t rsrc_t;
__device__ __forceinline__ rsrc_t mk_rsrc(const GAS char* p, int bytes) {
  return __builtin_amdgcn_make_buffer_rsrc((void*)p, 0, bytes, 0x00020000);
}
template <int AUX> __device__ __forceinline__ u32x4 bld(rsrc_t r, int voff, int soff) {
  return __builtin_amdgcn_raw_buffer_load_b128(r, voff, soff, AUX);
}
#define PIN(x) asm volatile("" : "+v"(x))

__device__ __forceinline__ bool tagok(u32x4 v, unsigned t0, unsigned t1) { return v.y == t0 && v.w == t1; }
__device__ __forceinline__ u32x4 gld(rsrc_t rs, int voff, int soff) { return __builtin_amdgcn_raw_buffer_load_b128(rs, voff, soff, SC1); }
__device__ __forceinline__ void gst(rsrc_t rs, int voff, unsigned a, unsigned b, unsigned t0, unsigned t1) {
  const u32x4 g = {a, t0, b, t1};
  __builtin_amdgcn_raw_buffer_store_b128(g, rs, voff, 0, SC1);
}
#define FENCE() asm volatile("" ::: "memory")
#define PSLEEP 2
template <int N> __device__ __forceinline__ void gather(rsrc_t rs, int (&vo)[N], f32x2 (&v)[N], unsigned t0, unsigned t1) {
#pragma unroll
  for (int i = 0; i < N; ++i) { v[i].x = 0.f; v[i].y = 0.f; }
  for (unsigned spins = 0;; ++spins) {
    FENCE();
    u32x4 g[N];
#pragma unroll
    for (int i = 0; i < N; ++i) g[i] = gld(rs, vo[i], 0);
    bool more = false;
#pragma unroll
    for (int i = 0; i < N; ++i) {
      const bool ok = tagok(g[i], t0, t1) && vo[i] != OOB;
      v[i].x = ok ? __uint_as_float(g[i].x) : v[i].x;
      v[i].y = ok ? __uint_as_float(g[i].z) : v[i].y;
      vo[i] = ok ? OOB : vo[i];
      more = more || vo[i] != OOB;
    }
    if (!__any(more) || spins > SPIN_CAP) break;
    if (PSLEEP) __builtin_amdgcn_s_sleep(PSLEEP);
  }
  FENCE();
}

__global__ void __launch_bounds__(NT, 4)
fused_decode(const GAS char* __restrict__ x_, const GAS char* __restrict__ kc, const GAS char* __restrict__ vc,
             const GAS char* __restrict__ wq, const GAS char* __restrict__ wo, const GAS char* __restrict__ wg,
             const GAS char* __restrict__ wu, const GAS char* __restrict__ wd, GAS char* __restrict__ out, char* ws) {
  __shared__ __attribute__((aligned(16))) float lds[L_END];
  STAMP_DECL;
  STAMP(0);
  int tid = threadIdx.x, lane = tid & 63;
  const int w = __builtin_amdgcn_readfirstlane(tid >> 6);
  int r = lane >> 4, c = lane & 15;
#define REID() do { asm volatile("" : "+v"(tid)); lane = tid & 63; r = lane >> 4; c = lane & 15; } while (0)
  const int bid = blockIdx.x, xcd = bid & 7, idx = bid >> 3;
  const int head = xcd * 4 + (idx >> 3), b = (idx >> 2) & 1, s = idx & 3;
  const rsrc_t rs = __builtin_amdgcn_make_buffer_rsrc((void*)ws, 0, WS_END, 0x00020000);
  const bool roleG = idx < 27;
  const int sub = idx & 7, lid = head * 8 + sub;
  volatile LAS unsigned* ctl = (volatile LAS unsigned*)&lds[L_CTL];
  const int l16 = lane * 16;

  if (tid < 4) ctl[tid] = 0u;
  __builtin_amdgcn_s_setprio(3);
  wg_barrier();
  if (w < 10) {
    const GAS unsigned* xdw = (const GAS unsigned*)x_;
    const int P = 40 * sub + 4 * w + r;
    const rsrc_t rq = mk_rsrc(wq + head * 256, (ABL & 1) ? 0 : D * 8192 - head * 256);
    unsigned xa[10];
#pragma unroll
    for (int k = 0; k < 10; ++k) xa[k] = xdw[(lane >> 5) * 320 + (lane & 31) + 32 * k];
    const unsigned xb0 = xdw[P], xb1 = xdw[320 + P];
    const u32x4 wq0 = bld<WAUX>(rq, (2 * P) * 8192 + c * 16, 0);
    const u32x4 wq1 = bld<WAUX>(rq, (2 * P) * 8192 + c * 16, 8192);
    const u32x2 ep = __builtin_amdgcn_raw_buffer_load_b64(rs, w == 0 ? WS_EPOCH : OOB, 0, SC1);
    SB();
    STAMP(1);
    const unsigned ones = 0x3F803F80u;
    float sm = 0.f;
#pragma unroll
    for (int k = 0; k < 10; ++k) sm = dot2(xa[k], ones, sm);
    sm = row_sum(sm); sm = tr16(sm, sm);
    const float mean = sm * (1.f / D);
    float sq = 0.f, sq2 = 0.f;
#pragma unroll
    for (int k = 0; k < 10; ++k) { const float a = bflo(xa[k]) - mean, bb = bfhi(xa[k]) - mean; sq = fmaf(a, a, sq); sq2 = fmaf(bb, bb, sq2); }
    sq += sq2;
    sq = row_sum(sq); sq = tr16(sq, sq);
    const float var = sq * (1.f / D);
    const float mb = bfr(mean), rstd = __builtin_amdgcn_rcpf(bfr(__builtin_amdgcn_sqrtf(bfr(bfr(var) + 1e-5f))));
    const float m0 = rdl(mb, 0), m1 = rdl(mb, 32), d0 = rdl(rstd, 0), d1 = rdl(rstd, 32);
    const unsigned xn0 = cvtpk(bfr(bflo(xb0) - m0) * d0, bfr(bfhi(xb0) - m0) * d0);
    const unsigned xn1 = cvtpk(bfr(bflo(xb1) - m1) * d1, bfr(bfhi(xb1) - m1) * d1);
    float a0[8], a1[8];
#pragma unroll
    for (int j = 0; j < 4; ++j) {
      const unsigned lo = plo(wq0[j], wq1[j]), hi = phi(wq0[j], wq1[j]);
      a0[2 * j] = dot2(lo, xn0, 0.f); a0[2 * j + 1] = dot2(hi, xn0, 0.f);
      a1[2 * j] = dot2(lo, xn1, 0.f); a1[2 * j + 1] = dot2(hi, xn1, 0.f);
    }
    float s0[4], s1[4];
#pragma unroll
    for (int i = 0; i < 4; ++i) { s0[i] = tr32(a0[i], a0[i + 4]); s1[i] = tr32(a1[i], a1[i + 4]); }
    f32x2 u0 = {tr16(s0[0], s0[2]), tr16(s0[1], s0[3])}, u1 = {tr16(s1[0], s1[2]), tr16(s1[1], s1[3])};
    *(f32x2*)&lds[L_PART + w * 256 + 8 * c + 2 * r] = u0;
    *(f32x2*)&lds[L_PART + w * 256 + 128 + 8 * c + 2 * r] = u1;
    FENCE();
    if (lane == 0) __hip_atomic_fetch_add((LAS unsigned*)&ctl[0], 1u, __ATOMIC_RELAXED, __HIP_MEMORY_SCOPE_WORKGROUP);
    STAMP(2);
    if (w == 0) {
      const unsigned long long e64 = ((unsigned long long)ep.y << 32) | ep.x;
      const unsigned long long tg = e64 * 6364136223846793005ull + 1442695040888963407ull;
      const unsigned t0 = (unsigned)tg, t1 = (unsigned)(tg >> 32);
      if (lane == 0) { ctl[4] = t0; ctl[5] = t1; }
      while (ctl[0] < 10u) {}
      FENCE();
      f32x2 p0 = {0.f, 0.f}, p1 = {0.f, 0.f};
#pragma unroll
      for (int ww = 0; ww < 10; ++ww) {
        p0 += *(const f32x2*)&lds[L_PART + ww * 256 + 2 * lane];
        p1 += *(const f32x2*)&lds[L_PART + ww * 256 + 128 + 2 * lane];
      }
      gst(rs, WS_QX + lid * 2048 + l16, __float_as_uint(p0.x), __float_as_uint(p0.y), t0, t1);
      gst(rs, WS_QX + lid * 2048 + 1024 + l16, __float_as_uint(p1.x), __float_as_uint(p1.y), t0, t1);
      STAMP(3);
      f32x2 qs;
      for (unsigned spins = 0;; ++spins) {
        FENCE();
        u32x4 qq[8];
#pragma unroll
        for (int k = 0; k < 8; ++k) qq[k] = gld(rs, WS_QX + head * 16384 + b * 1024 + l16, k * 2048);
        bool ok = true;
        qs.x = 0.f; qs.y = 0.f;
#pragma unroll
        for (int k = 0; k < 8; ++k) {
          ok = ok && tagok(qq[k], t0, t1);
          qs.x += __uint_as_float(qq[k].x); qs.y += __uint_as_float(qq[k].z);
        }
        if (__all(ok) || spins > SPIN_CAP) break;
      }
      ((unsigned*)lds)[L_QPK + lane] = cvtpk(qs.x, qs.y);
      FENCE();
      if (lane == 0) ctl[1] = 1u;
    }
  }
  SB();
  REID();
  __builtin_amdgcn_s_setprio(2);
  const size_t kvoff = ((size_t)((b * NH + head) * SK + s * 1024 + w * 64)) * 256;
  const rsrc_t rk = mk_rsrc(kc + kvoff, 16384), rv = mk_rsrc(vc + kvoff, 16384);
#define LDKV(R, t) bld<NTP>(R, l16, (t) * 1024)
  u32x4 kr[16], vr[16], tw[2], gwt[2];
  {
    const int tt = w < 10 ? tid : tid - 640;
    const int Q = tt / 10, ch = tt - Q * 10;
    const bool isd = w >= 10;
    const rsrc_t rt = mk_rsrc(isd ? wd + (size_t)idx * (64 * 1280) + xcd * 160 : wo + (size_t)head * (128 * 1280) + sub * 160,
                              ((ABL & 4) || w == 15 || (isd && !roleG)) ? 0 : 128 * 1280 - 1120);
    const int vo = (2 * Q) * 1280 + ch * 16;
    const GAS char* gp = ((lane & 8) ? wu : wg) + (size_t)(80 * xcd + 8 * w + 2 * r) * (FF * 2) + idx * 128 + (lane & 7) * 16;
    const bool gact = roleG && w < 10 && !(ABL & 2);
    if (!gact) gp = wg;
#pragma unroll
    for (int t = 0; t < 16; ++t) {
      kr[t] = LDKV(rk, t);
      if (t == 2) tw[0] = bld<WAUX>(rt, vo, 0);
      if (t == 5) gwt[0] = __builtin_nontemporal_load((const GAS u32x4*)gp);
      if (t == 8) tw[1] = bld<WAUX>(rt, vo, 1280);
      if (t == 11) gwt[1] = __builtin_nontemporal_load((const GAS u32x4*)(gp + FF * 2));
      SB();
    }
  }
  while (ctl[1] == 0u) __builtin_amdgcn_s_sleep(1);
  FENCE();
  REID();
  const u32x4 qv = *(const u32x4*)&lds[L_QPK + 4 * c];
  const unsigned tag0 = __builtin_amdgcn_readfirstlane(ctl[4]), tag1 = __builtin_amdgcn_readfirstlane(ctl[5]);
  STAMP(4);

  float sc;
  {
    float l1[2], l2[2], l3[2], l4[2];
#pragma unroll
    for (int t = 0; t < 16; ++t) {
      const u32x4 kk = kr[t];
      float d = dot2(kk[0], qv[0], 0.f);
      d = dot2(kk[1], qv[1], d); d = dot2(kk[2], qv[2], d); d = dot2(kk[3], qv[3], d);
      l1[t & 1] = d;
      if ((t & 1) == 1) l2[(t >> 1) & 1] = trd<X8>(l1[0], l1[1], (c & 8) != 0);
      if ((t & 3) == 3) l3[(t >> 2) & 1] = trd<X7>(l2[0], l2[1], (c & 4) != 0);
      if ((t & 7) == 7) l4[(t >> 3) & 1] = trd<X2>(l3[0], l3[1], (c & 2) != 0);
      SB();
    }
    sc = trd<X1>(l4[0], l4[1], (c & 1) != 0);
  }
  const float s2 = bfr(bfr(sc) * 0.08838834764831845f) * 1.4426950408889634f;
  unsigned ppk;
  {
    const float mw = wave_max(s2);
    const float p = __builtin_amdgcn_exp2f(s2 - mw);
    ppk = cvtpk(p, dppf<X8>(p));
    const float lw = wave_sum(p);
    unsigned old = 0u;
    if (lane == 0) {
      f32x2 t = {mw, lw}; *(f32x2*)&lds[L_WML + 2 * w] = t;
      FENCE();
      old = __hip_atomic_fetch_add((LAS unsigned*)&ctl[2], 1u, __ATOMIC_RELAXED, __HIP_MEMORY_SCOPE_WORKGROUP);
    }
    if (__builtin_amdgcn_readfirstlane(old) == 15u) {
      FENCE();
      const f32x2 ml = *(const f32x2*)&lds[L_WML + 2 * c];
      const float Mwg = row_max(ml.x);
      const float Lwg = row_sum(ml.y * __builtin_amdgcn_exp2f(ml.x - Mwg));
      if (lane == 0) gst(rs, WS_ML + ((b * NH + head) * 4 + s) * 16, __float_as_uint(Mwg), __float_as_uint(Lwg), tag0, tag1);
    }
  }
  STAMP(5);
  SB();
  REID();
  __builtin_amdgcn_s_setprio(1);
#pragma unroll
  for (int t = 0; t < 8; ++t) vr[t] = LDKV(rv, t);
  SB();
  {
    u32x4 lo, hi;
#pragma unroll
    for (int j = 0; j < 4; ++j) { lo[j] = plo(tw[0][j], tw[1][j]); hi[j] = phi(tw[0][j], tw[1][j]); }
    const int tt = w < 10 ? tid : tid - 640;
    const int Q = tt / 10, ch = tt - Q * 10;
    if (w < 15) {
      const int o = w < 10 ? L_WO + (ch * 64 + Q) * 4 : L_WD + (ch * 32 + Q) * 4;
      *(u32x4*)&lds[o] = lo;
      *(u32x4*)&lds[o + (w < 10 ? 2560 : 1280)] = hi;
    }
#pragma unroll
    for (int j = 0; j < 4; ++j) { lo[j] = plo(gwt[0][j], gwt[1][j]); hi[j] = phi(gwt[0][j], gwt[1][j]); }
    gwt[0] = lo; gwt[1] = hi;
    PIN(gwt[0]); PIN(gwt[1]);
    if (roleG && w < 10) {
      const unsigned ones = 0x3F803F80u;
      float v[8];
#pragma unroll
      for (int j = 0; j < 4; ++j) { v[2 * j] = dot2(lo[j], ones, 0.f); v[2 * j + 1] = dot2(hi[j], ones, 0.f); }
      float s4[4];
#pragma unroll
      for (int i = 0; i < 4; ++i) s4[i] = tr32(v[i], v[i + 4]);
      const f32x2 u = {tr16(s4[0], s4[2]), tr16(s4[1], s4[3])};
      *(f32x2*)&lds[L_CSP + w * 128 + 2 * lane] = u;
      FENCE();
      unsigned old = 0u;
      if (lane == 0) old = __hip_atomic_fetch_add((LAS unsigned*)&ctl[3], 1u, __ATOMIC_RELAXED, __HIP_MEMORY_SCOPE_WORKGROUP);
      if (__builtin_amdgcn_readfirstlane(old) == 9u) {
        FENCE();
        f32x2 t = {0.f, 0.f};
#pragma unroll
        for (int ww = 0; ww < 10; ++ww) t += *(const f32x2*)&lds[L_CSP + ww * 128 + 2 * lane];
        gst(rs, WS_CS + (idx * 8 + xcd) * 1024 + l16, __float_as_uint(t.x), __float_as_uint(t.y), tag0, tag1);
      }
    }
  }
  SB();
  __builtin_amdgcn_s_setprio(0);
#pragma unroll
  for (int t = 8; t < 16; ++t) vr[t] = LDKV(rv, t);
  SB();
  REID();
  const int mloff = w == 0 ? WS_ML + ((b * NH + head) * 4 + (lane & 3)) * 16 : OOB;
  u32x4 mls;
  {
    float a[8];
#pragma unroll
    for (int i = 0; i < 8; ++i) a[i] = 0.f;
#pragma unroll
    for (int u = 0; u < 8; ++u) {
      const u32x4 v0 = vr[2 * u], v1 = vr[2 * u + 1];
      unsigned pp;
      switch (u) {
        case 0: pp = dppu<BC + 0>(ppk); break;
        case 1: pp = dppu<BC + 4>(ppk); break;
        case 2: pp = dppu<BC + 2>(ppk); break;
        case 3: pp = dppu<BC + 6>(ppk); break;
        case 4: pp = dppu<BC + 1>(ppk); break;
        case 5: pp = dppu<BC + 5>(ppk); break;
        case 6: pp = dppu<BC + 3>(ppk); break;
        default: pp = dppu<BC + 7>(ppk); break;
      }
#pragma unroll
      for (int j = 0; j < 4; ++j) {
        a[2 * j] = dot2(plo(v0[j], v1[j]), pp, a[2 * j]);
        a[2 * j + 1] = dot2(phi(v0[j], v1[j]), pp, a[2 * j + 1]);
      }
#pragma unroll
      for (int i = 0; i < 8; ++i) PIN(a[i]);
      SB();
      if (u == 3) mls = gld(rs, mloff, 0);
      SB();
    }
    REID();
    float s4[4];
#pragma unroll
    for (int i = 0; i < 4; ++i) s4[i] = tr32(a[i], a[i + 4]);
    f32x2 u = {tr16(s4[0], s4[2]), tr16(s4[1], s4[3])};
    *(f32x2*)&lds[L_PART + w * 128 + 8 * c + 2 * r] = u;
  }
  STAMP(7);
  float wgt = 0.f;
  if (w == 0) {
    for (unsigned spins = 0; !__all(tagok(mls, tag0, tag1)) && spins < SPIN_CAP; ++spins) {
      FENCE();
      mls = gld(rs, mloff, 0);
    }
    FENCE();
    const float ms = __uint_as_float(mls.x), ls = __uint_as_float(mls.z);
    float M = fmaxf(ms, dppf<X1>(ms)); M = fmaxf(M, dppf<X2>(M));
    float L = ls * __builtin_amdgcn_exp2f(ms - M);
    L += dppf<X1>(L); L += dppf<X2>(L);
    wgt = __builtin_amdgcn_exp2f(lds[L_WML + 2 * c] - M) / L;
  }
  STAMP(6);
  wg_barrier();
  if (w == 0) {
    f32x2 o2 = {0.f, 0.f};
#pragma unroll
    for (int ww = 0; ww < 16; ++ww) o2 += *(const f32x2*)&lds[L_PART + ww * 128 + 2 * lane] * rdl(wgt, ww);
    gst(rs, WS_OX + lid * 1024 + l16, __float_as_uint(o2.x), __float_as_uint(o2.y), tag0, tag1);
  }
  STAMP(8);
  if (w >= 8) {
    const int bb = (w >> 2) & 1, Q = (w & 3) * 16 + (lane >> 2);
    int vo[1] = {WS_OX + (head * 8 + bb * 4 + (lane & 3)) * 1024 + Q * 16};
    f32x2 g[1];
    gather<1>(rs, vo, g, tag0, tag1);
    float o0 = g[0].x, o1 = g[0].y;
    o0 += dppf<X1>(o0); o1 += dppf<X1>(o1);
    o0 += dppf<X2>(o0); o1 += dppf<X2>(o1);
    if ((lane & 3) == 0) ((unsigned*)lds)[L_OPK + Q * 2 + bb] = cvtpk(o0, o1);
  }
  u32x4 wl, wh;
  if (w < 10) { wl = *(const u32x4*)&lds[L_WO + tid * 4]; wh = *(const u32x4*)&lds[L_WO + 2560 + tid * 4]; PIN(wl); PIN(wh); }
  unsigned xr = 0u;
  if (roleG && w < 10) xr = *(const GAS unsigned*)(x_ + ((lane >> 3) & 1) * (D * 2) + xcd * 160 + w * 16 + r * 4);
  wg_barrier();
  STAMP(9);
  if (w < 10) {
    const u32x2 op = *(const u32x2*)&lds[L_OPK + lane * 2];
    float v[16];
#pragma unroll
    for (int j = 0; j < 4; ++j) {
      v[2 * j] = dot2(wl[j], op.x, 0.f); v[2 * j + 1] = dot2(wh[j], op.x, 0.f);
      v[8 + 2 * j] = dot2(wl[j], op.y, 0.f); v[8 + 2 * j + 1] = dot2(wh[j], op.y, 0.f);
    }
    float s8[8], s4[4];
#pragma unroll
    for (int i = 0; i < 8; ++i) s8[i] = tr32(v[i], v[i + 8]);
#pragma unroll
    for (int i = 0; i < 4; ++i) s4[i] = tr16(s8[i], s8[i + 4]);
    float t0 = trd<X8>(s4[0], s4[2], (c & 8) != 0), t1 = trd<X8>(s4[1], s4[3], (c & 8) != 0);
    t0 += dppf<X1>(t0); t1 += dppf<X1>(t1);
    t0 += dppf<X2>(t0); t1 += dppf<X2>(t1);
    t0 += dppf<X7>(t0); t1 += dppf<X7>(t1);
    if ((lane & 7) == 0) gst(rs, WS_Y + ((sub * 10 + w) * 32 + head) * 128 + lane * 2, __float_as_uint(t0), __float_as_uint(t1), tag0, tag1);
  }
  STAMP(10);
  if (roleG) {
    const int gsel = (((lane >> 3) & 1) * 4 + r) * 16;
    float h0 = 0.f, h1 = 0.f;
    if (w >= 8) {
      int vo[1] = {WS_CS + (idx * 8 + (lane & 7)) * 1024 + (w - 8) * 128 + (lane >> 3) * 16};
      f32x2 g[1];
      gather<1>(rs, vo, g, tag0, tag1);
      f32x2 t = g[0];
      t.x += dppf<X1>(t.x); t.y += dppf<X1>(t.y);
      t.x += dppf<X2>(t.x); t.y += dppf<X2>(t.y);
      t.x += dppf<X7>(t.x); t.y += dppf<X7>(t.y);
      if ((lane & 7) == 0) *(f32x2*)&lds[L_CS + ((w - 8) * 8 + (lane >> 3)) * 2] = t;
    }
    if (w < 10) {
      const int vb = WS_Y + ((xcd * 10 + w) * 32 + (lane & 7)) * 128 + gsel;
      int vo[4] = {vb, vb + 1024, vb + 2048, vb + 3072};
      f32x2 g[4];
      gather<4>(rs, vo, g, tag0, tag1);
      STAMP(11);
      float a0 = (g[0].x + g[1].x) + (g[2].x + g[3].x), a1 = (g[0].y + g[1].y) + (g[2].y + g[3].y);
      a0 += dppf<X1>(a0); a1 += dppf<X1>(a1);
      a0 += dppf<X2>(a0); a1 += dppf<X2>(a1);
      a0 += dppf<X7>(a0); a1 += dppf<X7>(a1);
      { const unsigned ab = cvtpk(a0, a1); a0 = bflo(ab); a1 = bfhi(ab); }
      const unsigned pk = cvtpk(bflo(xr) + a0, bfhi(xr) + a1);
      h0 = bflo(pk); h1 = bfhi(pk);
      const unsigned po = dppu<X8>(pk);
      const unsigned hb0 = (lane & 8) ? po : pk, hb1 = (lane & 8) ? pk : po;
      float v[16];
#pragma unroll
      for (int j = 0; j < 4; ++j) {
        v[2 * j] = dot2(gwt[0][j], hb0, 0.f); v[2 * j + 1] = dot2(gwt[1][j], hb0, 0.f);
        v[8 + 2 * j] = dot2(gwt[0][j], hb1, 0.f); v[8 + 2 * j + 1] = dot2(gwt[1][j], hb1, 0.f);
      }
      float st = tr32(h0 + h1, dot2(pk, pk, 0.f));
      float s8[8];
#pragma unroll
      for (int i = 0; i < 8; ++i) s8[i] = tr32(v[i], v[i + 8]);
      st = tr16(st, st);
      f32x4 s4;
#pragma unroll
      for (int i = 0; i < 4; ++i) s4[i] = tr16(s8[i], s8[i + 4]);
      *(f32x4*)&lds[L_PART + tid * 4] = s4;
      if ((lane & 23) == 0) lds[L_ST + w * 4 + ((lane >> 3) & 1) * 2 + (lane >> 5)] = st;
    }
    wg_barrier();
    if (tid < 130) {
      const int o = tid < 128 ? L_PART + tid * 2 : L_ST + (tid - 128) * 2, st = tid < 128 ? 256 : 4;
      f32x2 t = {0.f, 0.f};
#pragma unroll
      for (int ww = 0; ww < 10; ++ww) t += *(const f32x2*)&lds[o + ww * st];
      gst(rs, WS_GP + (idx * 8 + xcd) * GP_REC + tid * 16, __float_as_uint(t.x), __float_as_uint(t.y), tag0, tag1);
    }
    STAMP(12);
    if (w < 10) { wl = *(const u32x4*)&lds[L_WD + (w * 32 + (lane & 31)) * 4]; wh = *(const u32x4*)&lds[L_WD + 1280 + (w * 32 + (lane & 31)) * 4]; PIN(wl); PIN(wh); }
    {
      const int wl2 = (w + 3) & 15;
      const int vb = WS_GP + (idx * 8 + (lane & 7)) * GP_REC + (lane >> 3) * 16;
      const bool st2 = w == 5 && lane < 16;
      int vo[2] = {vb + wl2 * 128, st2 ? vb + 2048 : OOB};
      f32x2 g[2];
      gather<2>(rs, vo, g, tag0, tag1);
      f32x2 t = g[0], t2 = g[1];
      t.x += dppf<X1>(t.x); t.y += dppf<X1>(t.y); t2.x += dppf<X1>(t2.x); t2.y += dppf<X1>(t2.y);
      t.x += dppf<X2>(t.x); t.y += dppf<X2>(t.y); t2.x += dppf<X2>(t2.x); t2.y += dppf<X2>(t2.y);
      t.x += dppf<X7>(t.x); t.y += dppf<X7>(t.y); t2.x += dppf<X7>(t2.x); t2.y += dppf<X7>(t2.y);
      if ((lane & 7) == 0) {
        *(f32x2*)&lds[L_G2 + (wl2 * 8 + (lane >> 3)) * 2] = t;
        if (st2) *(f32x2*)&lds[L_G2 + (128 + (lane >> 3)) * 2] = t2;
      }
    }
    wg_barrier();
    STAMP(13);
    if (w < 10) {
      const int k8 = (lane >> 2) & 7, n2 = (lane >> 1) & 1, n1 = lane & 1, bb = lane >> 5;
      const int tg = bb * 64 + n2 * 32 + k8 * 2 + n1, tc = n2 * 32 + n1 * 16 + k8;
      const f32x2 g = *(const f32x2*)&lds[L_G2 + tg * 2], u = *(const f32x2*)&lds[L_G2 + (tg + 16) * 2];
      const f32x2 cg = *(const f32x2*)&lds[L_CS + tc * 2], cu = *(const f32x2*)&lds[L_CS + (tc + 8) * 2];
      const f32x2 st = *(const f32x2*)&lds[L_G2 + (128 + bb) * 2];
      const float mean = st.x * (1.f / D);
      const float var = fmaxf(fmaf(-mean, mean, st.y * (1.f / D)), 0.f);
      const float mb = bfr(mean), rstd = __builtin_amdgcn_rcpf(bfr(__builtin_amdgcn_sqrtf(bfr(bfr(var) + 1e-5f))));
      const unsigned gb = cvtpk(fmaf(-mb, cg.x, g.x) * rstd, fmaf(-mb, cg.y, g.y) * rstd);
      const unsigned ub = cvtpk(fmaf(-mb, cu.x, u.x) * rstd, fmaf(-mb, cu.y, u.y) * rstd);
      const unsigned hh = cvtpk(bflo(gb) * bflo(ub), bfhi(gb) * bfhi(ub));
      float v[8];
#pragma unroll
      for (int j = 0; j < 4; ++j) { v[2 * j] = dot2(wl[j], hh, 0.f); v[2 * j + 1] = dot2(wh[j], hh, 0.f); }
      float s4[4];
#pragma unroll
      for (int i = 0; i < 4; ++i) s4[i] = tr16(v[i], v[i + 4]);
      float t0 = trd<X8>(s4[0], s4[2], (c & 8) != 0), t1 = trd<X8>(s4[1], s4[3], (c & 8) != 0);
      t0 += dppf<X1>(t0); t1 += dppf<X1>(t1);
      t0 += dppf<X2>(t0); t1 += dppf<X2>(t1);
      t0 += dppf<X7>(t0); t1 += dppf<X7>(t1);
      if ((lane & 7) == 0) gst(rs, WS_OP + ((xcd * 10 + w) * 32 + idx) * 128 + lane * 2, __float_as_uint(t0), __float_as_uint(t1), tag0, tag1);
      STAMP(14);
      if (w == idx) {
        const int vb = WS_OP + ((xcd * 10 + w) * 32 + (lane & 7)) * 128 + gsel;
        int vo[4] = {vb, vb + 1024, vb + 2048, (lane & 7) < 3 ? vb + 3072 : OOB};
        f32x2 g4[4];
        gather<4>(rs, vo, g4, tag0, tag1);
        float a0 = (g4[0].x + g4[1].x) + (g4[2].x + g4[3].x), a1 = (g4[0].y + g4[1].y) + (g4[2].y + g4[3].y);
        a0 += dppf<X1>(a0); a1 += dppf<X1>(a1);
        a0 += dppf<X2>(a0); a1 += dppf<X2>(a1);
        a0 += dppf<X7>(a0); a1 += dppf<X7>(a1);
        { const unsigned ab = cvtpk(a0, a1); a0 = bflo(ab); a1 = bfhi(ab); }
        if ((lane & 7) == 0) *(GAS unsigned*)(out + ((lane >> 3) & 1) * (D * 2) + xcd * 160 + w * 16 + r * 4) = cvtpk(h0 + a0, h1 + a1);
        if (bid == 0 && lane == 0) {
          const u32x2 tg2 = {tag0, tag1};
          __builtin_amdgcn_raw_buffer_store_b64(tg2, rs, WS_EPOCH, 0, SC1);
        }
        STAMP(15);
      }
    }
  }
  STAMP_OUT();
}

__global__ void dummy_small(char* ws) { if (ws == nullptr) __builtin_trap(); }
extern "C" void kernel_launch(void* const* d_in, const int* in_sizes, int n_in, void* d_out, int out_size, void* d_ws,
                              size_t ws_size, hipStream_t stream) {
  (void)in_sizes; (void)n_in; (void)out_size; (void)ws_size;
  fused_decode<<<NWG, NT, 0, stream>>>((const GAS char*)d_in[0], (const GAS char*)d_in[1], (const GAS char*)d_in[2],
                                       (const GAS char*)d_in[3], (const GAS char*)d_in[4], (const GAS char*)d_in[6],
                                       (const GAS char*)d_in[7], (const GAS char*)d_in[8], (GAS char*)d_out, (char*)d_ws);
  dummy_small<<<1, 64, 0, stream>>>((char*)d_ws);
}
```

```cpp
#define KAUX 2
#define VAUX 2
#define WAUX 0
#include <hip/hip_runtime.h>
#include <stdint.h>

#define MIMIC 1

typedef unsigned u32x4 __attribute__((ext_vector_type(4)));
typedef unsigned u32x2 __attribute__((ext_vector_type(2)));
typedef float f32x4 __attribute__((ext_vector_type(4)));
typedef float f32x2 __attribute__((ext_vector_type(2)));
typedef __bf16 bf16x2 __attribute__((ext_vector_type(2)));
#define GAS __attribute__((address_space(1)))
#define LAS __attribute__((address_space(3)))
#define PREF 1
#define ABL 0
#define PRIO 1

constexpr int D = 640, NH = 32, SK = 4096, DH = 128, FF = 1728;
constexpr int NWG = 256, NT = 1024;
constexpr unsigned SPIN_CAP = 1u << 22;

constexpr int WS_EPOCH = 0;
constexpr int WS_QX = 256;
constexpr int WS_ML = WS_QX + 256 * 2048;
constexpr int WS_OX = WS_ML + 4096;
constexpr int WS_Y = WS_OX + 256 * 1024;
constexpr int GP_REC = 17 * 128;
constexpr int WS_GP = WS_Y + 80 * 4096;
constexpr int WS_CS = WS_GP + 216 * GP_REC;
constexpr int WS_OP = WS_CS + 216 * 1024;
constexpr int WS_END = WS_OP + 80 * 4096;
static_assert(WS_OX % 128 == 0 && WS_Y % 128 == 0 && WS_GP % 128 == 0 && WS_OP % 128 == 0, "line alignment");

constexpr int L_PART = 0;
constexpr int L_QPK = L_PART + 2560;
constexpr int L_WML = L_QPK + 64;
constexpr int L_CTL = L_WML + 32;
constexpr int L_OPK = L_CTL + 8;
constexpr int L_ST = L_OPK + 128;
constexpr int L_G2 = L_ST + 40;
constexpr int L_CSP = L_G2 + 272;
constexpr int L_CS = L_CSP + 1280;
constexpr int L_WO = L_CS + 128;
constexpr int L_WD = L_WO + 5120;
constexpr int L_END = L_WD + 2560;

__device__ __forceinline__ float dot2(unsigned a, unsigned b, float c) {
  return __builtin_amdgcn_fdot2_f32_bf16(__builtin_bit_cast(bf16x2, a), __builtin_bit_cast(bf16x2, b), c, false);
}
__device__ __forceinline__ unsigned cvtpk(float lo, float hi) {
  bf16x2 v = {(__bf16)lo, (__bf16)hi};
  return __builtin_bit_cast(unsigned, v);
}
__device__ __forceinline__ float bflo(unsigned u) { return __builtin_bit_cast(float, u << 16); }
__device__ __forceinline__ float bfhi(unsigned u) { return __builtin_bit_cast(float, u & 0xffff0000u); }
__device__ __forceinline__ float bfr(float f) { return bflo(cvtpk(f, f)); }
__device__ __forceinline__ unsigned plo(unsigned a, unsigned b) { return __builtin_amdgcn_perm(b, a, 0x05040100u); }
__device__ __forceinline__ unsigned phi(unsigned a, unsigned b) { return __builtin_amdgcn_perm(b, a, 0x07060302u); }
template <int CTRL> __device__ __forceinline__ float dppf(float x) {
  return __builtin_bit_cast(float, __builtin_amdgcn_mov_dpp(__builtin_bit_cast(int, x), CTRL, 0xf, 0xf, true));
}
template <int CTRL> __device__ __forceinline__ unsigned dppu(unsigned x) {
  return (unsigned)__builtin_amdgcn_mov_dpp((int)x, CTRL, 0xf, 0xf, true);
}
constexpr int X1 = 0xB1, X2 = 0x4E, X7 = 0x141, X8 = 0x128, ROR4 = 0x124, ROR2 = 0x122, ROR1 = 0x121, BC = 0x150;
__device__ __forceinline__ float tr32(float a, float b) {
  auto s = __builtin_amdgcn_permlane32_swap(__float_as_uint(a), __float_as_uint(b), false, false);
  return __uint_as_float(s[0]) + __uint_as_float(s[1]);
}
__device__ __forceinline__ float tr16(float a, float b) {
  auto s = __builtin_amdgcn_permlane16_swap(__float_as_uint(a), __float_as_uint(b), false, false);
  return __uint_as_float(s[0]) + __uint_as_float(s[1]);
}
__device__ __forceinline__ float mx32(float a) {
  auto s = __builtin_amdgcn_permlane32_swap(__float_as_uint(a), __float_as_uint(a), false, false);
  return fmaxf(__uint_as_float(s[0]), __uint_as_float(s[1]));
}
__device__ __forceinline__ float mx16(float a) {
  auto s = __builtin_amdgcn_permlane16_swap(__float_as_uint(a), __float_as_uint(a), false, false);
  return fmaxf(__uint_as_float(s[0]), __uint_as_float(s[1]));
}
template <int CTRL> __device__ __forceinline__ float trd(float a, float b, bool sel) {
  const float t1 = a + dppf<CTRL>(a), t2 = b + dppf<CTRL>(b);
  return sel ? t2 : t1;
}
__device__ __forceinline__ float row_sum(float x) {
  x += dppf<X8>(x); x += dppf<ROR4>(x); x += dppf<X2>(x); x += dppf<X1>(x); return x;
}
__device__ __forceinline__ float row_max(float x) {
  x = fmaxf(x, dppf<X8>(x)); x = fmaxf(x, dppf<ROR4>(x)); x = fmaxf(x, dppf<X2>(x)); x = fmaxf(x, dppf<X1>(x)); return x;
}
__device__ __forceinline__ float wave_sum(float x) { x = row_sum(x); x = tr16(x, x); return tr32(x, x); }
__device__ __forceinline__ float wave_max(float x) { x = row_max(x); x = mx16(x); return mx32(x); }
__device__ __forceinline__ float rdl(float x, int l) { return __int_as_float(__builtin_amdgcn_readlane(__float_as_int(x), l)); }
__device__ __forceinline__ void wg_barrier() { asm volatile("s_waitcnt lgkmcnt(0)\n\ts_barrier" ::: "memory"); }
#define SB() __builtin_amdgcn_sched_barrier(0)
#define STAMP(i)
#define STAMP_DECL
#define STAMP_OUT()

constexpr int SC1 = 16, NTP = 2;
constexpr int OOB = 0x40000000;
typedef __amdgpu_buffer_rsrc_t rsrc_t;
__device__ __forceinline__ rsrc_t mk_rsrc(const GAS char* p, int bytes) {
  return __builtin_amdgcn_make_buffer_rsrc((void*)p, 0, bytes, 0x00020000);
}
template <int AUX> __device__ __forceinline__ u32x4 bld(rsrc_t r, int voff, int soff) {
  return __builtin_amdgcn_raw_buffer_load_b128(r, voff, soff, AUX);
}
#define PIN(x) asm volatile("" : "+v"(x))

__device__ __forceinline__ bool tagok(u32x4 v, unsigned t0, unsigned t1) { return v.y == t0 && v.w == t1; }
__device__ __forceinline__ u32x4 gld(rsrc_t rs, int voff, int soff) { return __builtin_amdgcn_raw_buffer_load_b128(rs, voff, soff, SC1); }
__device__ __forceinline__ void gst(rsrc_t rs, int voff, unsigned a, unsigned b, unsigned t0, unsigned t1) {
  const u32x4 g = {a, t0, b, t1};
  __builtin_amdgcn_raw_buffer_store_b128(g, rs, voff, 0, SC1);
}
#define FENCE() asm volatile("" ::: "memory")
#define PSLEEP 2
template <int N> __device__ __forceinline__ void gather(rsrc_t rs, int (&vo)[N], f32x2 (&v)[N], unsigned t0, unsigned t1) {
#pragma unroll
  for (int i = 0; i < N; ++i) { v[i].x = 0.f; v[i].y = 0.f; }
  for (unsigned spins = 0;; ++spins) {
    FENCE();
    u32x4 g[N];
#pragma unroll
    for (int i = 0; i < N; ++i) g[i] = gld(rs, vo[i], 0);
    bool more = false;
#pragma unroll
    for (int i = 0; i < N; ++i) {
      const bool ok = tagok(g[i], t0, t1) && vo[i] != OOB;
      v[i].x = ok ? __uint_as_float(g[i].x) : v[i].x;
      v[i].y = ok ? __uint_as_float(g[i].z) : v[i].y;
      vo[i] = ok ? OOB : vo[i];
      more = more || vo[i] != OOB;
    }
    if (!__any(more) || spins > SPIN_CAP) break;
    if (PSLEEP) __builtin_amdgcn_s_sleep(PSLEEP);
  }
  FENCE();
}

__global__ void __launch_bounds__(NT, 4)
fused_decode(const GAS char* __restrict__ x_, const GAS char* __restrict__ kc, const GAS char* __restrict__ vc,
             const GAS char* __restrict__ wq, const GAS char* __restrict__ wo, const GAS char* __restrict__ wg,
             const GAS char* __restrict__ wu, const GAS char* __restrict__ wd, GAS char* __restrict__ out, char* ws) {
  __shared__ __attribute__((aligned(16))) float lds[L_END];
  STAMP_DECL;
  STAMP(0);
  int tid = threadIdx.x, lane = tid & 63;
  const int w = __builtin_amdgcn_readfirstlane(tid >> 6);
  int r = lane >> 4, c = lane & 15;
#define REID() do { asm volatile("" : "+v"(tid)); lane = tid & 63; r = lane >> 4; c = lane & 15; } while (0)
  const int bid = blockIdx.x, xcd = bid & 7, idx = bid >> 3;
  const int head = xcd * 4 + (idx >> 3), b = (idx >> 2) & 1, s = idx & 3;
  const rsrc_t rs = __builtin_amdgcn_make_buffer_rsrc((void*)ws, 0, WS_END, 0x00020000);
  const bool roleG = idx < 27;
  const int sub = idx & 7, lid = head * 8 + sub;
  volatile LAS unsigned* ctl = (volatile LAS unsigned*)&lds[L_CTL];
  const int l16 = lane * 16;

  if (tid < 4) ctl[tid] = 0u;
  __builtin_amdgcn_s_setprio(3);
  wg_barrier();
  if (w < 10) {
    const GAS unsigned* xdw = (const GAS unsigned*)x_;
    const int P = 40 * sub + 4 * w + r;
    const rsrc_t rq = mk_rsrc(wq + head * 256, (ABL & 1) ? 0 : D * 8192 - head * 256);
    unsigned xa[10];
#pragma unroll
    for (int k = 0; k < 10; ++k) xa[k] = xdw[(lane >> 5) * 320 + (lane & 31) + 32 * k];
    const unsigned xb0 = xdw[P], xb1 = xdw[320 + P];
    const u32x4 wq0 = bld<WAUX>(rq, (2 * P) * 8192 + c * 16, 0);
    const u32x4 wq1 = bld<WAUX>(rq, (2 * P) * 8192 + c * 16, 8192);
    const u32x2 ep = __builtin_amdgcn_raw_buffer_load_b64(rs, w == 0 ? WS_EPOCH : OOB, 0, SC1);
    SB();
    STAMP(1);
    const unsigned ones = 0x3F803F80u;
    float sm = 0.f;
#pragma unroll
    for (int k = 0; k < 10; ++k) sm = dot2(xa[k], ones, sm);
    sm = row_sum(sm); sm = tr16(sm, sm);
    const float mean = sm * (1.f / D);
    float sq = 0.f, sq2 = 0.f;
#pragma unroll
    for (int k = 0; k < 10; ++k) { const float a = bflo(xa[k]) - mean, bb = bfhi(xa[k]) - mean; sq = fmaf(a, a, sq); sq2 = fmaf(bb, bb, sq2); }
    sq += sq2;
    sq = row_sum(sq); sq = tr16(sq, sq);
    const float var = sq * (1.f / D);
    const float mb = bfr(mean), rstd = __builtin_amdgcn_rcpf(bfr(__builtin_amdgcn_sqrtf(bfr(bfr(var) + 1e-5f))));
    const float m0 = rdl(mb, 0), m1 = rdl(mb, 32), d0 = rdl(rstd, 0), d1 = rdl(rstd, 32);
    const unsigned xn0 = cvtpk(bfr(bflo(xb0) - m0) * d0, bfr(bfhi(xb0) - m0) * d0);
    const unsigned xn1 = cvtpk(bfr(bflo(xb1) - m1) * d1, bfr(bfhi(xb1) - m1) * d1);
    float a0[8], a1[8];
#pragma unroll
    for (int j = 0; j < 4; ++j) {
      const unsigned lo = plo(wq0[j], wq1[j]), hi = phi(wq0[j], wq1[j]);
      a0[2 * j] = dot2(lo, xn0, 0.f); a0[2 * j + 1] = dot2(hi, xn0, 0.f);
      a1[2 * j] = dot2(lo, xn1, 0.f); a1[2 * j + 1] = dot2(hi, xn1, 0.f);
    }
    float s0[4], s1[4];
#pragma unroll
    for (int i = 0; i < 4; ++i) { s0[i] = tr32(a0[i], a0[i + 4]); s1[i] = tr32(a1[i], a1[i + 4]); }
    f32x2 u0 = {tr16(s0[0], s0[2]), tr16(s0[1], s0[3])}, u1 = {tr16(s1[0], s1[2]), tr16(s1[1], s1[3])};
    *(f32x2*)&lds[L_PART + w * 256 + 8 * c + 2 * r] = u0;
    *(f32x2*)&lds[L_PART + w * 256 + 128 + 8 * c + 2 * r] = u1;
    FENCE();
    if (lane == 0) __hip_atomic_fetch_add((LAS unsigned*)&ctl[0], 1u, __ATOMIC_RELAXED, __HIP_MEMORY_SCOPE_WORKGROUP);
    STAMP(2);
    if (w == 0) {
      const unsigned long long e64 = ((unsigned long long)ep.y << 32) | ep.x;
      const unsigned long long tg = e64 * 6364136223846793005ull + 1442695040888963407ull;
      const unsigned t0 = (unsigned)tg, t1 = (unsigned)(tg >> 32);
      if (lane == 0) { ctl[4] = t0; ctl[5] = t1; }
      while (ctl[0] < 10u) {}
      FENCE();
      f32x2 p0 = {0.f, 0.f}, p1 = {0.f, 0.f};
#pragma unroll
      for (int ww = 0; ww < 10; ++ww) {
        p0 += *(const f32x2*)&lds[L_PART + ww * 256 + 2 * lane];
        p1 += *(const f32x2*)&lds[L_PART + ww * 256 + 128 + 2 * lane];
      }
      gst(rs, WS_QX + lid * 2048 + l16, __float_as_uint(p0.x), __float_as_uint(p0.y), t0, t1);
      gst(rs, WS_QX + lid * 2048 + 1024 + l16, __float_as_uint(p1.x), __float_as_uint(p1.y), t0, t1);
      STAMP(3);
      f32x2 qs;
      for (unsigned spins = 0;; ++spins) {
        FENCE();
        u32x4 qq[8];
#pragma unroll
        for (int k = 0; k < 8; ++k) qq[k] = gld(rs, WS_QX + head * 16384 + b * 1024 + l16, k * 2048);
        bool ok = true;
        qs.x = 0.f; qs.y = 0.f;
#pragma unroll
        for (int k = 0; k < 8; ++k) {
          ok = ok && tagok(qq[k], t0, t1);
          qs.x += __uint_as_float(qq[k].x); qs.y += __uint_as_float(qq[k].z);
        }
        if (__all(ok) || spins > SPIN_CAP) break;
      }
      ((unsigned*)lds)[L_QPK + lane] = cvtpk(qs.x, qs.y);
      FENCE();
      if (lane == 0) ctl[1] = 1u;
    }
  }
  SB();
  REID();
  __builtin_amdgcn_s_setprio(2);
  const size_t kvoff = ((size_t)((b * NH + head) * SK + s * 1024 + w * 64)) * 256;
  const rsrc_t rk = mk_rsrc(kc + kvoff, 16384), rv = mk_rsrc(vc + kvoff, 16384);
#define LDK(t) bld<KAUX>(rk, l16, (t) * 1024)
#define LDV(t) bld<VAUX>(rv, l16, (t) * 1024)
  u32x4 kr[16], vr[16], tw[2], gwt[2];
  {
    const int tt = w < 10 ? tid : tid - 640;
    const int Q = tt / 10, ch = tt - Q * 10;
    const bool isd = w >= 10;
    const rsrc_t rt = mk_rsrc(isd ? wd + (size_t)idx * (64 * 1280) + xcd * 160 : wo + (size_t)head * (128 * 1280) + sub * 160,
                              ((ABL & 4) || w == 15 || (isd && !roleG)) ? 0 : 128 * 1280 - 1120);
    const int vo = (2 * Q) * 1280 + ch * 16;
    const GAS char* gp = ((lane & 8) ? wu : wg) + (size_t)(80 * xcd + 8 * w + 2 * r) * (FF * 2) + idx * 128 + (lane & 7) * 16;
    const bool gact = roleG && w < 10 && !(ABL & 2);
    if (!gact) gp = wg;
#pragma unroll
    for (int t = 0; t < 16; ++t) {
      kr[t] = LDK(t);
      if (t == 2) tw[0] = bld<WAUX>(rt, vo, 0);
      if (t == 5) gwt[0] = WAUX ? __builtin_nontemporal_load((const GAS u32x4*)gp) : *(const GAS u32x4*)gp;
      if (t == 8) tw[1] = bld<WAUX>(rt, vo, 1280);
      if (t == 11) gwt[1] = WAUX ? __builtin_nontemporal_load((const GAS u32x4*)(gp + FF * 2)) : *(const GAS u32x4*)(gp + FF * 2);
      SB();
    }
  }
  while (ctl[1] == 0u) __builtin_amdgcn_s_sleep(1);
  FENCE();
  REID();
  const u32x4 qv = *(const u32x4*)&lds[L_QPK + 4 * c];
  const unsigned tag0 = __builtin_amdgcn_readfirstlane(ctl[4]), tag1 = __builtin_amdgcn_readfirstlane(ctl[5]);
  STAMP(4);

  float sc;
  {
    float l1[2], l2[2], l3[2], l4[2];
#pragma unroll
    for (int t = 0; t < 16; ++t) {
      const u32x4 kk = kr[t];
      float d = dot2(kk[0], qv[0], 0.f);
      d = dot2(kk[1], qv[1], d); d = dot2(kk[2], qv[2], d); d = dot2(kk[3], qv[3], d);
      l1[t & 1] = d;
      if ((t & 1) == 1) l2[(t >> 1) & 1] = trd<X8>(l1[0], l1[1], (c & 8) != 0);
      if ((t & 3) == 3) l3[(t >> 2) & 1] = trd<X7>(l2[0], l2[1], (c & 4) != 0);
      if ((t & 7) == 7) l4[(t >> 3) & 1] = trd<X2>(l3[0], l3[1], (c & 2) != 0);
      SB();
    }
    sc = trd<X1>(l4[0], l4[1], (c & 1) != 0);
  }
  const float s2 = bfr(bfr(sc) * 0.08838834764831845f) * 1.4426950408889634f;
  unsigned ppk;
  {
    const float mw = wave_max(s2);
    const float p = __builtin_amdgcn_exp2f(s2 - mw);
    ppk = cvtpk(p, dppf<X8>(p));
    const float lw = wave_sum(p);
    unsigned old = 0u;
    if (lane == 0) {
      f32x2 t = {mw, lw}; *(f32x2*)&lds[L_WML + 2 * w] = t;
      FENCE();
      old = __hip_atomic_fetch_add((LAS unsigned*)&ctl[2], 1u, __ATOMIC_RELAXED, __HIP_MEMORY_SCOPE_WORKGROUP);
    }
    if (__builtin_amdgcn_readfirstlane(old) == 15u) {
      FENCE();
      const f32x2 ml = *(const f32x2*)&lds[L_WML + 2 * c];
      const float Mwg = row_max(ml.x);
      const float Lwg = row_sum(ml.y * __builtin_amdgcn_exp2f(ml.x - Mwg));
      if (lane == 0) gst(rs, WS_ML + ((b * NH + head) * 4 + s) * 16, __float_as_uint(Mwg), __float_as_uint(Lwg), tag0, tag1);
    }
  }
  STAMP(5);
  SB();
  REID();
  __builtin_amdgcn_s_setprio(1);
#pragma unroll
  for (int t = 0; t < 8; ++t) vr[t] = LDV(t);
  SB();
  {
    u32x4 lo, hi;
#pragma unroll
    for (int j = 0; j < 4; ++j) { lo[j] = plo(tw[0][j], tw[1][j]); hi[j] = phi(tw[0][j], tw[1][j]); }
    const int tt = w < 10 ? tid : tid - 640;
    const int Q = tt / 10, ch = tt - Q * 10;
    if (w < 15) {
      const int o = w < 10 ? L_WO + (ch * 64 + Q) * 4 : L_WD + (ch * 32 + Q) * 4;
      *(u32x4*)&lds[o] = lo;
      *(u32x4*)&lds[o + (w < 10 ? 2560 : 1280)] = hi;
    }
#pragma unroll
    for (int j = 0; j < 4; ++j) { lo[j] = plo(gwt[0][j], gwt[1][j]); hi[j] = phi(gwt[0][j], gwt[1][j]); }
    gwt[0] = lo; gwt[1] = hi;
    PIN(gwt[0]); PIN(gwt[1]);
    if (roleG && w < 10) {
      const unsigned ones = 0x3F803F80u;
      float v[8];
#pragma unroll
      for (int j = 0; j < 4; ++j) { v[2 * j] = dot2(lo[j], ones, 0.f); v[2 * j + 1] = dot2(hi[j], ones, 0.f); }
      float s4[4];
#pragma unroll
      for (int i = 0; i < 4; ++i) s4[i] = tr32(v[i], v[i + 4]);
      const f32x2 u = {tr16(s4[0], s4[2]), tr16(s4[1], s4[3])};
      *(f32x2*)&lds[L_CSP + w * 128 + 2 * lane] = u;
      FENCE();
      unsigned old = 0u;
      if (lane == 0) old = __hip_atomic_fetch_add((LAS unsigned*)&ctl[3], 1u, __ATOMIC_RELAXED, __HIP_MEMORY_SCOPE_WORKGROUP);
      if (__builtin_amdgcn_readfirstlane(old) == 9u) {
        FENCE();
        f32x2 t = {0.f, 0.f};
#pragma unroll
        for (int ww = 0; ww < 10; ++ww) t += *(const f32x2*)&lds[L_CSP + ww * 128 + 2 * lane];
        gst(rs, WS_CS + (idx * 8 + xcd) * 1024 + l16, __float_as_uint(t.x), __float_as_uint(t.y), tag0, tag1);
      }
    }
  }
  SB();
  __builtin_amdgcn_s_setprio(0);
#pragma unroll
  for (int t = 8; t < 16; ++t) vr[t] = LDV(t);
  SB();
  REID();
  const int mloff = w == 0 ? WS_ML + ((b * NH + head) * 4 + (lane & 3)) * 16 : OOB;
  u32x4 mls;
  {
    float a[8];
#pragma unroll
    for (int i = 0; i < 8; ++i) a[i] = 0.f;
#pragma unroll
    for (int u = 0; u < 8; ++u) {
      const u32x4 v0 = vr[2 * u], v1 = vr[2 * u + 1];
      unsigned pp;
      switch (u) {
        case 0: pp = dppu<BC + 0>(ppk); break;
        case 1: pp = dppu<BC + 4>(ppk); break;
        case 2: pp = dppu<BC + 2>(ppk); break;
        case 3: pp = dppu<BC + 6>(ppk); break;
        case 4: pp = dppu<BC + 1>(ppk); break;
        case 5: pp = dppu<BC + 5>(ppk); break;
        case 6: pp = dppu<BC + 3>(ppk); break;
        default: pp = dppu<BC + 7>(ppk); break;
      }
#pragma unroll
      for (int j = 0; j < 4; ++j) {
        a[2 * j] = dot2(plo(v0[j], v1[j]), pp, a[2 * j]);
        a[2 * j + 1] = dot2(phi(v0[j], v1[j]), pp, a[2 * j + 1]);
      }
#pragma unroll
      for (int i = 0; i < 8; ++i) PIN(a[i]);
      SB();
      if (u == 3) mls = gld(rs, mloff, 0);
      SB();
    }
    REID();
    float s4[4];
#pragma unroll
    for (int i = 0; i < 4; ++i) s4[i] = tr32(a[i], a[i + 4]);
    f32x2 u = {tr16(s4[0], s4[2]), tr16(s4[1], s4[3])};
    *(f32x2*)&lds[L_PART + w * 128 + 8 * c + 2 * r] = u;
  }
  STAMP(7);
  float wgt = 0.f;
  if (w == 0) {
    for (unsigned spins = 0; !__all(tagok(mls, tag0, tag1)) && spins < SPIN_CAP; ++spins) {
      FENCE();
      mls = gld(rs, mloff, 0);
    }
    FENCE();
    const float ms = __uint_as_float(mls.x), ls = __uint_as_float(mls.z);
    float M = fmaxf(ms, dppf<X1>(ms)); M = fmaxf(M, dppf<X2>(M));
    float L = ls * __builtin_amdgcn_exp2f(ms - M);
    L += dppf<X1>(L); L += dppf<X2>(L);
    wgt = __builtin_amdgcn_exp2f(lds[L_WML + 2 * c] - M) / L;
  }
  STAMP(6);
  wg_barrier();
  if (w == 0) {
    f32x2 o2 = {0.f, 0.f};
#pragma unroll
    for (int ww = 0; ww < 16; ++ww) o2 += *(const f32x2*)&lds[L_PART + ww * 128 + 2 * lane] * rdl(wgt, ww);
    gst(rs, WS_OX + lid * 1024 + l16, __float_as_uint(o2.x), __float_as_uint(o2.y), tag0, tag1);
  }
  STAMP(8);
  if (w >= 8) {
    const int bb = (w >> 2) & 1, Q = (w & 3) * 16 + (lane >> 2);
    int vo[1] = {WS_OX + (head * 8 + bb * 4 + (lane & 3)) * 1024 + Q * 16};
    f32x2 g[1];
    gather<1>(rs, vo, g, tag0, tag1);
    float o0 = g[0].x, o1 = g[0].y;
    o0 += dppf<X1>(o0); o1 += dppf<X1>(o1);
    o0 += dppf<X2>(o0); o1 += dppf<X2>(o1);
    if ((lane & 3) == 0) ((unsigned*)lds)[L_OPK + Q * 2 + bb] = cvtpk(o0, o1);
  }
  u32x4 wl, wh;
  if (w < 10) { wl = *(const u32x4*)&lds[L_WO + tid * 4]; wh = *(const u32x4*)&lds[L_WO + 2560 + tid * 4]; PIN(wl); PIN(wh); }
  unsigned xr = 0u;
  if (roleG && w < 10) xr = *(const GAS unsigned*)(x_ + ((lane >> 3) & 1) * (D * 2) + xcd * 160 + w * 16 + r * 4);
  wg_barrier();
  STAMP(9);
  if (w < 10) {
    const u32x2 op = *(const u32x2*)&lds[L_OPK + lane * 2];
    float v[16];
#pragma unroll
    for (int j = 0; j < 4; ++j) {
      v[2 * j] = dot2(wl[j], op.x, 0.f); v[2 * j + 1] = dot2(wh[j], op.x, 0.f);
      v[8 + 2 * j] = dot2(wl[j], op.y, 0.f); v[8 + 2 * j + 1] = dot2(wh[j], op.y, 0.f);
    }
    float s8[8], s4[4];
#pragma unroll
    for (int i = 0; i < 8; ++i) s8[i] = tr32(v[i], v[i + 8]);
#pragma unroll
    for (int i = 0; i < 4; ++i) s4[i] = tr16(s8[i], s8[i + 4]);
    float t0 = trd<X8>(s4[0], s4[2], (c & 8) != 0), t1 = trd<X8>(s4[1], s4[3], (c & 8) != 0);
    t0 += dppf<X1>(t0); t1 += dppf<X1>(t1);
    t0 += dppf<X2>(t0); t1 += dppf<X2>(t1);
    t0 += dppf<X7>(t0); t1 += dppf<X7>(t1);
    if ((lane & 7) == 0) gst(rs, WS_Y + ((sub * 10 + w) * 32 + head) * 128 + lane * 2, __float_as_uint(t0), __float_as_uint(t1), tag0, tag1);
  }
  STAMP(10);
  if (roleG) {
    const int gsel = (((lane >> 3) & 1) * 4 + r) * 16;
    float h0 = 0.f, h1 = 0.f;
    if (w >= 8) {
      int vo[1] = {WS_CS + (idx * 8 + (lane & 7)) * 1024 + (w - 8) * 128 + (lane >> 3) * 16};
      f32x2 g[1];
      gather<1>(rs, vo, g, tag0, tag1);
      f32x2 t = g[0];
      t.x += dppf<X1>(t.x); t.y += dppf<X1>(t.y);
      t.x += dppf<X2>(t.x); t.y += dppf<X2>(t.y);
      t.x += dppf<X7>(t.x); t.y += dppf<X7>(t.y);
      if ((lane & 7) == 0) *(f32x2*)&lds[L_CS + ((w - 8) * 8 + (lane >> 3)) * 2] = t;
    }
    if (w < 10) {
      const int vb = WS_Y + ((xcd * 10 + w) * 32 + (lane & 7)) * 128 + gsel;
      int vo[4] = {vb, vb + 1024, vb + 2048, vb + 3072};
      f32x2 g[4];
      gather<4>(rs, vo, g, tag0, tag1);
      STAMP(11);
      float a0 = (g[0].x + g[1].x) + (g[2].x + g[3].x), a1 = (g[0].y + g[1].y) + (g[2].y + g[3].y);
      a0 += dppf<X1>(a0); a1 += dppf<X1>(a1);
      a0 += dppf<X2>(a0); a1 += dppf<X2>(a1);
      a0 += dppf<X7>(a0); a1 += dppf<X7>(a1);
      { const unsigned ab = cvtpk(a0, a1); a0 = bflo(ab); a1 = bfhi(ab); }
      const unsigned pk = cvtpk(bflo(xr) + a0, bfhi(xr) + a1);
      h0 = bflo(pk); h1 = bfhi(pk);
      const unsigned po = dppu<X8>(pk);
      const unsigned hb0 = (lane & 8) ? po : pk, hb1 = (lane & 8) ? pk : po;
      float v[16];
#pragma unroll
      for (int j = 0; j < 4; ++j) {
        v[2 * j] = dot2(gwt[0][j], hb0, 0.f); v[2 * j + 1] = dot2(gwt[1][j], hb0, 0.f);
        v[8 + 2 * j] = dot2(gwt[0][j], hb1, 0.f); v[8 + 2 * j + 1] = dot2(gwt[1][j], hb1, 0.f);
      }
      float st = tr32(h0 + h1, dot2(pk, pk, 0.f));
      float s8[8];
#pragma unroll
      for (int i = 0; i < 8; ++i) s8[i] = tr32(v[i], v[i + 8]);
      st = tr16(st, st);
      f32x4 s4;
#pragma unroll
      for (int i = 0; i < 4; ++i) s4[i] = tr16(s8[i], s8[i + 4]);
      *(f32x4*)&lds[L_PART + tid * 4] = s4;
      if ((lane & 23) == 0) lds[L_ST + w * 4 + ((lane >> 3) & 1) * 2 + (lane >> 5)] = st;
    }
    wg_barrier();
    if (tid < 130) {
      const int o = tid < 128 ? L_PART + tid * 2 : L_ST + (tid - 128) * 2, st = tid < 128 ? 256 : 4;
      f32x2 t = {0.f, 0.f};
#pragma unroll
      for (int ww = 0; ww < 10; ++ww) t += *(const f32x2*)&lds[o + ww * st];
      gst(rs, WS_GP + (idx * 8 + xcd) * GP_REC + tid * 16, __float_as_uint(t.x), __float_as_uint(t.y), tag0, tag1);
    }
    STAMP(12);
    if (w < 10) { wl = *(const u32x4*)&lds[L_WD + (w * 32 + (lane & 31)) * 4]; wh = *(const u32x4*)&lds[L_WD + 1280 + (w * 32 + (lane & 31)) * 4]; PIN(wl); PIN(wh); }
    {
      const int wl2 = (w + 3) & 15;
      const int vb = WS_GP + (idx * 8 + (lane & 7)) * GP_REC + (lane >> 3) * 16;
      const bool st2 = w == 5 && lane < 16;
      int vo[2] = {vb + wl2 * 128, st2 ? vb + 2048 : OOB};
      f32x2 g[2];
      gather<2>(rs, vo, g, tag0, tag1);
      f32x2 t = g[0], t2 = g[1];
      t.x += dppf<X1>(t.x); t.y += dppf<X1>(t.y); t2.x += dppf<X1>(t2.x); t2.y += dppf<X1>(t2.y);
      t.x += dppf<X2>(t.x); t.y += dppf<X2>(t.y); t2.x += dppf<X2>(t2.x); t2.y += dppf<X2>(t2.y);
      t.x += dppf<X7>(t.x); t.y += dppf<X7>(t.y); t2.x += dppf<X7>(t2.x); t2.y += dppf<X7>(t2.y);
      if ((lane & 7) == 0) {
        *(f32x2*)&lds[L_G2 + (wl2 * 8 + (lane >> 3)) * 2] = t;
        if (st2) *(f32x2*)&lds[L_G2 + (128 + (lane >> 3)) * 2] = t2;
      }
    }
    wg_barrier();
    STAMP(13);
    if (w < 10) {
      const int k8 = (lane >> 2) & 7, n2 = (lane >> 1) & 1, n1 = lane & 1, bb = lane >> 5;
      const int tg = bb * 64 + n2 * 32 + k8 * 2 + n1, tc = n2 * 32 + n1 * 16 + k8;
      const f32x2 g = *(const f32x2*)&lds[L_G2 + tg * 2], u = *(const f32x2*)&lds[L_G2 + (tg + 16) * 2];
      const f32x2 cg = *(const f32x2*)&lds[L_CS + tc * 2], cu = *(const f32x2*)&lds[L_CS + (tc + 8) * 2];
      const f32x2 st = *(const f32x2*)&lds[L_G2 + (128 + bb) * 2];
      const float mean = st.x * (1.f / D);
      const float var = fmaxf(fmaf(-mean, mean, st.y * (1.f / D)), 0.f);
      const float mb = bfr(mean), rstd = __builtin_amdgcn_rcpf(bfr(__builtin_amdgcn_sqrtf(bfr(bfr(var) + 1e-5f))));
      const unsigned gb = cvtpk(fmaf(-mb, cg.x, g.x) * rstd, fmaf(-mb, cg.y, g.y) * rstd);
      const unsigned ub = cvtpk(fmaf(-mb, cu.x, u.x) * rstd, fmaf(-mb, cu.y, u.y) * rstd);
      const unsigned hh = cvtpk(bflo(gb) * bflo(ub), bfhi(gb) * bfhi(ub));
      float v[8];
#pragma unroll
      for (int j = 0; j < 4; ++j) { v[2 * j] = dot2(wl[j], hh, 0.f); v[2 * j + 1] = dot2(wh[j], hh, 0.f); }
      float s4[4];
#pragma unroll
      for (int i = 0; i < 4; ++i) s4[i] = tr16(v[i], v[i + 4]);
      float t0 = trd<X8>(s4[0], s4[2], (c & 8) != 0), t1 = trd<X8>(s4[1], s4[3], (c & 8) != 0);
      t0 += dppf<X1>(t0); t1 += dppf<X1>(t1);
      t0 += dppf<X2>(t0); t1 += dppf<X2>(t1);
      t0 += dppf<X7>(t0); t1 += dppf<X7>(t1);
      if ((lane & 7) == 0) gst(rs, WS_OP + ((xcd * 10 + w) * 32 + idx) * 128 + lane * 2, __float_as_uint(t0), __float_as_uint(t1), tag0, tag1);
      STAMP(14);
      if (w == idx) {
        const int vb = WS_OP + ((xcd * 10 + w) * 32 + (lane & 7)) * 128 + gsel;
        int vo[4] = {vb, vb + 1024, vb + 2048, (lane & 7) < 3 ? vb + 3072 : OOB};
        f32x2 g4[4];
        gather<4>(rs, vo, g4, tag0, tag1);
        float a0 = (g4[0].x + g4[1].x) + (g4[2].x + g4[3].x), a1 = (g4[0].y + g4[1].y) + (g4[2].y + g4[3].y);
        a0 += dppf<X1>(a0); a1 += dppf<X1>(a1);
        a0 += dppf<X2>(a0); a1 += dppf<X2>(a1);
        a0 += dppf<X7>(a0); a1 += dppf<X7>(a1);
        { const unsigned ab = cvtpk(a0, a1); a0 = bflo(ab); a1 = bfhi(ab); }
        if ((lane & 7) == 0) *(GAS unsigned*)(out + ((lane >> 3) & 1) * (D * 2) + xcd * 160 + w * 16 + r * 4) = cvtpk(h0 + a0, h1 + a1);
        if (bid == 0 && lane == 0) {
          const u32x2 tg2 = {tag0, tag1};
          __builtin_amdgcn_raw_buffer_store_b64(tg2, rs, WS_EPOCH, 0, SC1);
        }
        STAMP(15);
      }
    }
  }
  STAMP_OUT();
}

extern "C" void kernel_launch(void* const* d_in, const int* in_sizes, int n_in, void* d_out, int out_size, void* d_ws,
                              size_t ws_size, hipStream_t stream) {
  (void)in_sizes; (void)n_in; (void)out_size; (void)ws_size;
  fused_decode<<<NWG, NT, 0, stream>>>((const GAS char*)d_in[0], (const GAS char*)d_in[1], (const GAS char*)d_in[2],
                                       (const GAS char*)d_in[3], (const GAS char*)d_in[4], (const GAS char*)d_in[6],
                                       (const GAS char*)d_in[7], (const GAS char*)d_in[8], (GAS char*)d_out, (char*)d_ws);
}
```

```cpp
#define STAMPS 1
#include <hip/hip_runtime.h>
#include <stdint.h>

#define MIMIC 1

typedef unsigned u32x4 __attribute__((ext_vector_type(4)));
typedef unsigned u32x2 __attribute__((ext_vector_type(2)));
typedef float f32x4 __attribute__((ext_vector_type(4)));
typedef float f32x2 __attribute__((ext_vector_type(2)));
typedef __bf16 bf16x2 __attribute__((ext_vector_type(2)));
#define GAS __attribute__((address_space(1)))
#define LAS __attribute__((address_space(3)))
#define WAUX 2
#define PREF 1
#define KAUX 2
#define VAUX 2
#define ABL 0
#define PRIO 1

constexpr int D = 640, NH = 32, SK = 4096, DH = 128, FF = 1728;
constexpr int NWG = 256, NT = 1024;
constexpr unsigned SPIN_CAP = 1u << 22;

constexpr int WS_EPOCH = 0;
constexpr int WS_QX = 256;
constexpr int WS_ML = WS_QX + 256 * 2048;
constexpr int WS_OX = WS_ML + 4096;
constexpr int WS_Y = WS_OX + 256 * 1024;
constexpr int GP_REC = 17 * 128;
constexpr int WS_GP = WS_Y + 80 * 4096;
constexpr int WS_CS = WS_GP + 216 * GP_REC;
constexpr int WS_OP = WS_CS + 216 * 1024;
constexpr int WS_END = WS_OP + 80 * 4096;
static_assert(WS_OX % 128 == 0 && WS_Y % 128 == 0 && WS_GP % 128 == 0 && WS_OP % 128 == 0, "line alignment");

constexpr int L_PART = 0;
constexpr int L_QPK = L_PART + 2560;
constexpr int L_WML = L_QPK + 64;
constexpr int L_CTL = L_WML + 32;
constexpr int L_OPK = L_CTL + 8;
constexpr int L_ST = L_OPK + 128;
constexpr int L_G2 = L_ST + 40;
constexpr int L_CSP = L_G2 + 272;
constexpr int L_CS = L_CSP + 1280;
constexpr int L_WO = L_CS + 128;
constexpr int L_WD = L_WO + 5120;
constexpr int L_END = L_WD + 2560;

__device__ __forceinline__ float dot2(unsigned a, unsigned b, float c) {
  return __builtin_amdgcn_fdot2_f32_bf16(__builtin_bit_cast(bf16x2, a), __builtin_bit_cast(bf16x2, b), c, false);
}
__device__ __forceinline__ unsigned cvtpk(float lo, float hi) {
  bf16x2 v = {(__bf16)lo, (__bf16)hi};
  return __builtin_bit_cast(unsigned, v);
}
__device__ __forceinline__ float bflo(unsigned u) { return __builtin_bit_cast(float, u << 16); }
__device__ __forceinline__ float bfhi(unsigned u) { return __builtin_bit_cast(float, u & 0xffff0000u); }
__device__ __forceinline__ float bfr(float f) { return bflo(cvtpk(f, f)); }
__device__ __forceinline__ unsigned plo(unsigned a, unsigned b) { return __builtin_amdgcn_perm(b, a, 0x05040100u); }
__device__ __forceinline__ unsigned phi(unsigned a, unsigned b) { return __builtin_amdgcn_perm(b, a, 0x07060302u); }
template <int CTRL> __device__ __forceinline__ float dppf(float x) {
  return __builtin_bit_cast(float, __builtin_amdgcn_mov_dpp(__builtin_bit_cast(int, x), CTRL, 0xf, 0xf, true));
}
template <int CTRL> __device__ __forceinline__ unsigned dppu(unsigned x) {
  return (unsigned)__builtin_amdgcn_mov_dpp((int)x, CTRL, 0xf, 0xf, true);
}
constexpr int X1 = 0xB1, X2 = 0x4E, X7 = 0x141, X8 = 0x128, ROR4 = 0x124, ROR2 = 0x122, ROR1 = 0x121, BC = 0x150;
__device__ __forceinline__ float tr32(float a, float b) {
  auto s = __builtin_amdgcn_permlane32_swap(__float_as_uint(a), __float_as_uint(b), false, false);
  return __uint_as_float(s[0]) + __uint_as_float(s[1]);
}
__device__ __forceinline__ float tr16(float a, float b) {
  auto s = __builtin_amdgcn_permlane16_swap(__float_as_uint(a), __float_as_uint(b), false, false);
  return __uint_as_float(s[0]) + __uint_as_float(s[1]);
}
__device__ __forceinline__ float mx32(float a) {
  auto s = __builtin_amdgcn_permlane32_swap(__float_as_uint(a), __float_as_uint(a), false, false);
  return fmaxf(__uint_as_float(s[0]), __uint_as_float(s[1]));
}
__device__ __forceinline__ float mx16(float a) {
  auto s = __builtin_amdgcn_permlane16_swap(__float_as_uint(a), __float_as_uint(a), false, false);
  return fmaxf(__uint_as_float(s[0]), __uint_as_float(s[1]));
}
template <int CTRL> __device__ __forceinline__ float trd(float a, float b, bool sel) {
  const float t1 = a + dppf<CTRL>(a), t2 = b + dppf<CTRL>(b);
  return sel ? t2 : t1;
}
__device__ __forceinline__ float row_sum(float x) {
  x += dppf<X8>(x); x += dppf<ROR4>(x); x += dppf<X2>(x); x += dppf<X1>(x); return x;
}
__device__ __forceinline__ float row_max(float x) {
  x = fmaxf(x, dppf<X8>(x)); x = fmaxf(x, dppf<ROR4>(x)); x = fmaxf(x, dppf<X2>(x)); x = fmaxf(x, dppf<X1>(x)); return x;
}
__device__ __forceinline__ float wave_sum(float x) { x = row_sum(x); x = tr16(x, x); return tr32(x, x); }
__device__ __forceinline__ float wave_max(float x) { x = row_max(x); x = mx16(x); return mx32(x); }
__device__ __forceinline__ float rdl(float x, int l) { return __int_as_float(__builtin_amdgcn_readlane(__float_as_int(x), l)); }
__device__ __forceinline__ void wg_barrier() { asm volatile("s_waitcnt lgkmcnt(0)\n\ts_barrier" ::: "memory"); }
#define SB() __builtin_amdgcn_sched_barrier(0)
constexpr int NSTAMP = 16, WS_STAMP = 16 << 20;
#define STAMP(i) do { SB(); unsigned long long t_; asm volatile("s_memrealtime %0\n\ts_waitcnt lgkmcnt(0)" : "=s"(t_) :: "memory"); st_[i] = (unsigned)t_ | 1u; SB(); } while (0)
#define STAMP_DECL unsigned st_[NSTAMP]; _Pragma("unroll") for (int i_ = 0; i_ < NSTAMP; ++i_) st_[i_] = 0u
#define STAMP_OUT() do { if ((threadIdx.x & 63) == 0) { _Pragma("unroll") for (int i_ = 0; i_ < NSTAMP; ++i_) ((unsigned*)(ws + WS_STAMP))[(blockIdx.x * 16 + (threadIdx.x >> 6)) * NSTAMP + i_] = st_[i_]; } } while (0)

constexpr int SC1 = 16, NTP = 2;
constexpr int OOB = 0x40000000;
typedef __amdgpu_buffer_rsrc_t rsrc_t;
__device__ __forceinline__ rsrc_t mk_rsrc(const GAS char* p, int bytes) {
  return __builtin_amdgcn_make_buffer_rsrc((void*)p, 0, bytes, 0x00020000);
}
template <int AUX> __device__ __forceinline__ u32x4 bld(rsrc_t r, int voff, int soff) {
  return __builtin_amdgcn_raw_buffer_load_b128(r, voff, soff, AUX);
}
#define PIN(x) asm volatile("" : "+v"(x))

__device__ __forceinline__ bool tagok(u32x4 v, unsigned t0, unsigned t1) { return v.y == t0 && v.w == t1; }
__device__ __forceinline__ u32x4 gld(rsrc_t rs, int voff, int soff) { return __builtin_amdgcn_raw_buffer_load_b128(rs, voff, soff, SC1); }
__device__ __forceinline__ void gst(rsrc_t rs, int voff, unsigned a, unsigned b, unsigned t0, unsigned t1) {
  const u32x4 g = {a, t0, b, t1};
  __builtin_amdgcn_raw_buffer_store_b128(g, rs, voff, 0, SC1);
}
#define FENCE() asm volatile("" ::: "memory")
#define PSLEEP 2
template <int N> __device__ __forceinline__ void gather(rsrc_t rs, int (&vo)[N], f32x2 (&v)[N], unsigned t0, unsigned t1) {
#pragma unroll
  for (int i = 0; i < N; ++i) { v[i].x = 0.f; v[i].y = 0.f; }
  for (unsigned spins = 0;; ++spins) {
    FENCE();
    u32x4 g[N];
#pragma unroll
    for (int i = 0; i < N; ++i) g[i] = gld(rs, vo[i], 0);
    bool more = false;
#pragma unroll
    for (int i = 0; i < N; ++i) {
      const bool ok = tagok(g[i], t0, t1) && vo[i] != OOB;
      v[i].x = ok ? __uint_as_float(g[i].x) : v[i].x;
      v[i].y = ok ? __uint_as_float(g[i].z) : v[i].y;
      vo[i] = ok ? OOB : vo[i];
      more = more || vo[i] != OOB;
    }
    if (!__any(more) || spins > SPIN_CAP) break;
    if (PSLEEP) __builtin_amdgcn_s_sleep(PSLEEP);
  }
  FENCE();
}

__global__ void __launch_bounds__(NT, 4)
fused_decode(const GAS char* __restrict__ x_, const GAS char* __restrict__ kc, const GAS char* __restrict__ vc,
             const GAS char* __restrict__ wq, const GAS char* __restrict__ wo, const GAS char* __restrict__ wg,
             const GAS char* __restrict__ wu, const GAS char* __restrict__ wd, GAS char* __restrict__ out, char* ws) {
  __shared__ __attribute__((aligned(16))) float lds[L_END];
  STAMP_DECL;
  STAMP(0);
  int tid = threadIdx.x, lane = tid & 63;
  const int w = __builtin_amdgcn_readfirstlane(tid >> 6);
  int r = lane >> 4, c = lane & 15;
#define REID() do { asm volatile("" : "+v"(tid)); lane = tid & 63; r = lane >> 4; c = lane & 15; } while (0)
  const int bid = blockIdx.x, xcd = bid & 7, idx = bid >> 3;
  const int head = xcd * 4 + (idx >> 3), b = (idx >> 2) & 1, s = idx & 3;
  const rsrc_t rs = __builtin_amdgcn_make_buffer_rsrc((void*)ws, 0, WS_END, 0x00020000);
  const bool roleG = idx < 27;
  const int sub = idx & 7, lid = head * 8 + sub;
  volatile LAS unsigned* ctl = (volatile LAS unsigned*)&lds[L_CTL];
  const int l16 = lane * 16;

  if (tid < 4) ctl[tid] = 0u;
  __builtin_amdgcn_s_setprio(3);
  wg_barrier();
  if (w < 10) {
    const GAS unsigned* xdw = (const GAS unsigned*)x_;
    const int P = 40 * sub + 4 * w + r;
    const rsrc_t rq = mk_rsrc(wq + head * 256, (ABL & 1) ? 0 : D * 8192 - head * 256);
    unsigned xa[10];
#pragma unroll
    for (int k = 0; k < 10; ++k) xa[k] = xdw[(lane >> 5) * 320 + (lane & 31) + 32 * k];
    const unsigned xb0 = xdw[P], xb1 = xdw[320 + P];
    const u32x4 wq0 = bld<WAUX>(rq, (2 * P) * 8192 + c * 16, 0);
    const u32x4 wq1 = bld<WAUX>(rq, (2 * P) * 8192 + c * 16, 8192);
    const u32x2 ep = __builtin_amdgcn_raw_buffer_load_b64(rs, w == 0 ? WS_EPOCH : OOB, 0, SC1);
    SB();
    STAMP(1);
    const unsigned ones = 0x3F803F80u;
    float sm = 0.f;
#pragma unroll
    for (int k = 0; k < 10; ++k) sm = dot2(xa[k], ones, sm);
    sm = row_sum(sm); sm = tr16(sm, sm);
    const float mean = sm * (1.f / D);
    float sq = 0.f, sq2 = 0.f;
#pragma unroll
    for (int k = 0; k < 10; ++k) { const float a = bflo(xa[k]) - mean, bb = bfhi(xa[k]) - mean; sq = fmaf(a, a, sq); sq2 = fmaf(bb, bb, sq2); }
    sq += sq2;
    sq = row_sum(sq); sq = tr16(sq, sq);
    const float var = sq * (1.f / D);
    const float mb = bfr(mean), rstd = __builtin_amdgcn_rcpf(bfr(__builtin_amdgcn_sqrtf(bfr(bfr(var) + 1e-5f))));
    const float m0 = rdl(mb, 0), m1 = rdl(mb, 32), d0 = rdl(rstd, 0), d1 = rdl(rstd, 32);
    const unsigned xn0 = cvtpk(bfr(bflo(xb0) - m0) * d0, bfr(bfhi(xb0) - m0) * d0);
    const unsigned xn1 = cvtpk(bfr(bflo(xb1) - m1) * d1, bfr(bfhi(xb1) - m1) * d1);
    float a0[8], a1[8];
#pragma unroll
    for (int j = 0; j < 4; ++j) {
      const unsigned lo = plo(wq0[j], wq1[j]), hi = phi(wq0[j], wq1[j]);
      a0[2 * j] = dot2(lo, xn0, 0.f); a0[2 * j + 1] = dot2(hi, xn0, 0.f);
      a1[2 * j] = dot2(lo, xn1, 0.f); a1[2 * j + 1] = dot2(hi, xn1, 0.f);
    }
    float s0[4], s1[4];
#pragma unroll
    for (int i = 0; i < 4; ++i) { s0[i] = tr32(a0[i], a0[i + 4]); s1[i] = tr32(a1[i], a1[i + 4]); }
    f32x2 u0 = {tr16(s0[0], s0[2]), tr16(s0[1], s0[3])}, u1 = {tr16(s1[0], s1[2]), tr16(s1[1], s1[3])};
    *(f32x2*)&lds[L_PART + w * 256 + 8 * c + 2 * r] = u0;
    *(f32x2*)&lds[L_PART + w * 256 + 128 + 8 * c + 2 * r] = u1;
    FENCE();
    if (lane == 0) __hip_atomic_fetch_add((LAS unsigned*)&ctl[0], 1u, __ATOMIC_RELAXED, __HIP_MEMORY_SCOPE_WORKGROUP);
    STAMP(2);
    if (w == 0) {
      const unsigned long long e64 = ((unsigned long long)ep.y << 32) | ep.x;
      const unsigned long long tg = e64 * 6364136223846793005ull + 1442695040888963407ull;
      const unsigned t0 = (unsigned)tg, t1 = (unsigned)(tg >> 32);
      if (lane == 0) { ctl[4] = t0; ctl[5] = t1; }
      while (ctl[0] < 10u) {}
      FENCE();
      f32x2 p0 = {0.f, 0.f}, p1 = {0.f, 0.f};
#pragma unroll
      for (int ww = 0; ww < 10; ++ww) {
        p0 += *(const f32x2*)&lds[L_PART + ww * 256 + 2 * lane];
        p1 += *(const f32x2*)&lds[L_PART + ww * 256 + 128 + 2 * lane];
      }
      gst(rs, WS_QX + lid * 2048 + l16, __float_as_uint(p0.x), __float_as_uint(p0.y), t0, t1);
      gst(rs, WS_QX + lid * 2048 + 1024 + l16, __float_as_uint(p1.x), __float_as_uint(p1.y), t0, t1);
      STAMP(3);
      f32x2 qs;
      for (unsigned spins = 0;; ++spins) {
        FENCE();
        u32x4 qq[8];
#pragma unroll
        for (int k = 0; k < 8; ++k) qq[k] = gld(rs, WS_QX + head * 16384 + b * 1024 + l16, k * 2048);
        bool ok = true;
        qs.x = 0.f; qs.y = 0.f;
#pragma unroll
        for (int k = 0; k < 8; ++k) {
          ok = ok && tagok(qq[k], t0, t1);
          qs.x += __uint_as_float(qq[k].x); qs.y += __uint_as_float(qq[k].z);
        }
        if (__all(ok) || spins > SPIN_CAP) break;
      }
      ((unsigned*)lds)[L_QPK + lane] = cvtpk(qs.x, qs.y);
      FENCE();
      if (lane == 0) ctl[1] = 1u;
    }
  }
  SB();
  REID();
  __builtin_amdgcn_s_setprio(2);
  const size_t kvoff = ((size_t)((b * NH + head) * SK + s * 1024 + w * 64)) * 256;
  const rsrc_t rk = mk_rsrc(kc + kvoff, 16384), rv = mk_rsrc(vc + kvoff, 16384);
#define LDK(t) bld<KAUX>(rk, l16, (t) * 1024)
#define LDV(t) bld<VAUX>(rv, l16, (t) * 1024)
  u32x4 kr[16], vr[16], tw[2], gwt[2];
  {
    const int tt = w < 10 ? tid : tid - 640;
    const int Q = tt / 10, ch = tt - Q * 10;
    const bool isd = w >= 10;
    const rsrc_t rt = mk_rsrc(isd ? wd + (size_t)idx * (64 * 1280) + xcd * 160 : wo + (size_t)head * (128 * 1280) + sub * 160,
                              ((ABL & 4) || w == 15 || (isd && !roleG)) ? 0 : 128 * 1280 - 1120);
    const int vo = (2 * Q) * 1280 + ch * 16;
    const GAS char* gp = ((lane & 8) ? wu : wg) + (size_t)(80 * xcd + 8 * w + 2 * r) * (FF * 2) + idx * 128 + (lane & 7) * 16;
    const bool gact = roleG && w < 10 && !(ABL & 2);
    if (!gact) gp = wg;
#pragma unroll
    for (int t = 0; t < 16; ++t) {
      kr[t] = LDK(t);
      if (t == 2) tw[0] = bld<WAUX>(rt, vo, 0);
      if (t == 5) gwt[0] = WAUX ? __builtin_nontemporal_load((const GAS u32x4*)gp) : *(const GAS u32x4*)gp;
      if (t == 8) tw[1] = bld<WAUX>(rt, vo, 1280);
      if (t == 11) gwt[1] = WAUX ? __builtin_nontemporal_load((const GAS u32x4*)(gp + FF * 2)) : *(const GAS u32x4*)(gp + FF * 2);
      SB();
    }
  }
  while (ctl[1] == 0u) __builtin_amdgcn_s_sleep(1);
  FENCE();
  REID();
  const u32x4 qv = *(const u32x4*)&lds[L_QPK + 4 * c];
  const unsigned tag0 = __builtin_amdgcn_readfirstlane(ctl[4]), tag1 = __builtin_amdgcn_readfirstlane(ctl[5]);
  STAMP(4);

  float sc;
  {
    float l1[2], l2[2], l3[2], l4[2];
#pragma unroll
    for (int t = 0; t < 16; ++t) {
      const u32x4 kk = kr[t];
      float d = dot2(kk[0], qv[0], 0.f);
      d = dot2(kk[1], qv[1], d); d = dot2(kk[2], qv[2], d); d = dot2(kk[3], qv[3], d);
      l1[t & 1] = d;
      if ((t & 1) == 1) l2[(t >> 1) & 1] = trd<X8>(l1[0], l1[1], (c & 8) != 0);
      if ((t & 3) == 3) l3[(t >> 2) & 1] = trd<X7>(l2[0], l2[1], (c & 4) != 0);
      if ((t & 7) == 7) l4[(t >> 3) & 1] = trd<X2>(l3[0], l3[1], (c & 2) != 0);
      SB();
    }
    sc = trd<X1>(l4[0], l4[1], (c & 1) != 0);
  }
  const float s2 = bfr(bfr(sc) * 0.08838834764831845f) * 1.4426950408889634f;
  unsigned ppk;
  {
    const float mw = wave_max(s2);
    const float p = __builtin_amdgcn_exp2f(s2 - mw);
    ppk = cvtpk(p, dppf<X8>(p));
    const float lw = wave_sum(p);
    unsigned old = 0u;
    if (lane == 0) {
      f32x2 t = {mw, lw}; *(f32x2*)&lds[L_WML + 2 * w] = t;
      FENCE();
      old = __hip_atomic_fetch_add((LAS unsigned*)&ctl[2], 1u, __ATOMIC_RELAXED, __HIP_MEMORY_SCOPE_WORKGROUP);
    }
    if (__builtin_amdgcn_readfirstlane(old) == 15u) {
      FENCE();
      const f32x2 ml = *(const f32x2*)&lds[L_WML + 2 * c];
      const float Mwg = row_max(ml.x);
      const float Lwg = row_sum(ml.y * __builtin_amdgcn_exp2f(ml.x - Mwg));
      if (lane == 0) gst(rs, WS_ML + ((b * NH + head) * 4 + s) * 16, __float_as_uint(Mwg), __float_as_uint(Lwg), tag0, tag1);
    }
  }
  STAMP(5);
  SB();
  REID();
  __builtin_amdgcn_s_setprio(1);
#pragma unroll
  for (int t = 0; t < 8; ++t) vr[t] = LDV(t);
  SB();
  {
    u32x4 lo, hi;
#pragma unroll
    for (int j = 0; j < 4; ++j) { lo[j] = plo(tw[0][j], tw[1][j]); hi[j] = phi(tw[0][j], tw[1][j]); }
    const int tt = w < 10 ? tid : tid - 640;
    const int Q = tt / 10, ch = tt - Q * 10;
    if (w < 15) {
      const int o = w < 10 ? L_WO + (ch * 64 + Q) * 4 : L_WD + (ch * 32 + Q) * 4;
      *(u32x4*)&lds[o] = lo;
      *(u32x4*)&lds[o + (w < 10 ? 2560 : 1280)] = hi;
    }
#pragma unroll
    for (int j = 0; j < 4; ++j) { lo[j] = plo(gwt[0][j], gwt[1][j]); hi[j] = phi(gwt[0][j], gwt[1][j]); }
    gwt[0] = lo; gwt[1] = hi;
    PIN(gwt[0]); PIN(gwt[1]);
    if (roleG && w < 10) {
      const unsigned ones = 0x3F803F80u;
      float v[8];
#pragma unroll
      for (int j = 0; j < 4; ++j) { v[2 * j] = dot2(lo[j], ones, 0.f); v[2 * j + 1] = dot2(hi[j], ones, 0.f); }
      float s4[4];
#pragma unroll
      for (int i = 0; i < 4; ++i) s4[i] = tr32(v[i], v[i + 4]);
      const f32x2 u = {tr16(s4[0], s4[2]), tr16(s4[1], s4[3])};
      *(f32x2*)&lds[L_CSP + w * 128 + 2 * lane] = u;
      FENCE();
      unsigned old = 0u;
      if (lane == 0) old = __hip_atomic_fetch_add((LAS unsigned*)&ctl[3], 1u, __ATOMIC_RELAXED, __HIP_MEMORY_SCOPE_WORKGROUP);
      if (__builtin_amdgcn_readfirstlane(old) == 9u) {
        FENCE();
        f32x2 t = {0.f, 0.f};
#pragma unroll
        for (int ww = 0; ww < 10; ++ww) t += *(const f32x2*)&lds[L_CSP + ww * 128 + 2 * lane];
        gst(rs, WS_CS + (idx * 8 + xcd) * 1024 + l16, __float_as_uint(t.x), __float_as_uint(t.y), tag0, tag1);
      }
    }
  }
  SB();
  __builtin_amdgcn_s_setprio(0);
#pragma unroll
  for (int t = 8; t < 16; ++t) vr[t] = LDV(t);
  SB();
  REID();
  const int mloff = w == 0 ? WS_ML + ((b * NH + head) * 4 + (lane & 3)) * 16 : OOB;
  u32x4 mls;
  {
    float a[8];
#pragma unroll
    for (int i = 0; i < 8; ++i) a[i] = 0.f;
#pragma unroll
    for (int u = 0; u < 8; ++u) {
      const u32x4 v0 = vr[2 * u], v1 = vr[2 * u + 1];
      unsigned pp;
      switch (u) {
        case 0: pp = dppu<BC + 0>(ppk); break;
        case 1: pp = dppu<BC + 4>(ppk); break;
        case 2: pp = dppu<BC + 2>(ppk); break;
        case 3: pp = dppu<BC + 6>(ppk); break;
        case 4: pp = dppu<BC + 1>(ppk); break;
        case 5: pp = dppu<BC + 5>(ppk); break;
        case 6: pp = dppu<BC + 3>(ppk); break;
        default: pp = dppu<BC + 7>(ppk); break;
      }
#pragma unroll
      for (int j = 0; j < 4; ++j) {
        a[2 * j] = dot2(plo(v0[j], v1[j]), pp, a[2 * j]);
        a[2 * j + 1] = dot2(phi(v0[j], v1[j]), pp, a[2 * j + 1]);
      }
#pragma unroll
      for (int i = 0; i < 8; ++i) PIN(a[i]);
      SB();
      if (u == 3) mls = gld(rs, mloff, 0);
      SB();
    }
    REID();
    float s4[4];
#pragma unroll
    for (int i = 0; i < 4; ++i) s4[i] = tr32(a[i], a[i + 4]);
    f32x2 u = {tr16(s4[0], s4[2]), tr16(s4[1], s4[3])};
    *(f32x2*)&lds[L_PART + w * 128 + 8 * c + 2 * r] = u;
  }
  STAMP(7);
  float wgt = 0.f;
  if (w == 0) {
    for (unsigned spins = 0; !__all(tagok(mls, tag0, tag1)) && spins < SPIN_CAP; ++spins) {
      FENCE();
      mls = gld(rs, mloff, 0);
    }
    FENCE();
    const float ms = __uint_as_float(mls.x), ls = __uint_as_float(mls.z);
    float M = fmaxf(ms, dppf<X1>(ms)); M = fmaxf(M, dppf<X2>(M));
    float L = ls * __builtin_amdgcn_exp2f(ms - M);
    L += dppf<X1>(L); L += dppf<X2>(L);
    wgt = __builtin_amdgcn_exp2f(lds[L_WML + 2 * c] - M) / L;
  }
  STAMP(6);
  wg_barrier();
  if (w == 0) {
    f32x2 o2 = {0.f, 0.f};
#pragma unroll
    for (int ww = 0; ww < 16; ++ww) o2 += *(const f32x2*)&lds[L_PART + ww * 128 + 2 * lane] * rdl(wgt, ww);
    gst(rs, WS_OX + lid * 1024 + l16, __float_as_uint(o2.x), __float_as_uint(o2.y), tag0, tag1);
  }
  STAMP(8);
  if (w >= 8) {
    const int bb = (w >> 2) & 1, Q = (w & 3) * 16 + (lane >> 2);
    int vo[1] = {WS_OX + (head * 8 + bb * 4 + (lane & 3)) * 1024 + Q * 16};
    f32x2 g[1];
    gather<1>(rs, vo, g, tag0, tag1);
    float o0 = g[0].x, o1 = g[0].y;
    o0 += dppf<X1>(o0); o1 += dppf<X1>(o1);
    o0 += dppf<X2>(o0); o1 += dppf<X2>(o1);
    if ((lane & 3) == 0) ((unsigned*)lds)[L_OPK + Q * 2 + bb] = cvtpk(o0, o1);
  }
  u32x4 wl, wh;
  if (w < 10) { wl = *(const u32x4*)&lds[L_WO + tid * 4]; wh = *(const u32x4*)&lds[L_WO + 2560 + tid * 4]; PIN(wl); PIN(wh); }
  unsigned xr = 0u;
  if (roleG && w < 10) xr = *(const GAS unsigned*)(x_ + ((lane >> 3) & 1) * (D * 2) + xcd * 160 + w * 16 + r * 4);
  wg_barrier();
  STAMP(9);
  if (w < 10) {
    const u32x2 op = *(const u32x2*)&lds[L_OPK + lane * 2];
    float v[16];
#pragma unroll
    for (int j = 0; j < 4; ++j) {
      v[2 * j] = dot2(wl[j], op.x, 0.f); v[2 * j + 1] = dot2(wh[j], op.x, 0.f);
      v[8 + 2 * j] = dot2(wl[j], op.y, 0.f); v[8 + 2 * j + 1] = dot2(wh[j], op.y, 0.f);
    }
    float s8[8], s4[4];
#pragma unroll
    for (int i = 0; i < 8; ++i) s8[i] = tr32(v[i], v[i + 8]);
#pragma unroll
    for (int i = 0; i < 4; ++i) s4[i] = tr16(s8[i], s8[i + 4]);
    float t0 = trd<X8>(s4[0], s4[2], (c & 8) != 0), t1 = trd<X8>(s4[1], s4[3], (c & 8) != 0);
    t0 += dppf<X1>(t0); t1 += dppf<X1>(t1);
    t0 += dppf<X2>(t0); t1 += dppf<X2>(t1);
    t0 += dppf<X7>(t0); t1 += dppf<X7>(t1);
    if ((lane & 7) == 0) gst(rs, WS_Y + ((sub * 10 + w) * 32 + head) * 128 + lane * 2, __float_as_uint(t0), __float_as_uint(t1), tag0, tag1);
  }
  STAMP(10);
  if (roleG) {
    const int gsel = (((lane >> 3) & 1) * 4 + r) * 16;
    float h0 = 0.f, h1 = 0.f;
    if (w >= 8) {
      int vo[1] = {WS_CS + (idx * 8 + (lane & 7)) * 1024 + (w - 8) * 128 + (lane >> 3) * 16};
      f32x2 g[1];
      gather<1>(rs, vo, g, tag0, tag1);
      f32x2 t = g[0];
      t.x += dppf<X1>(t.x); t.y += dppf<X1>(t.y);
      t.x += dppf<X2>(t.x); t.y += dppf<X2>(t.y);
      t.x += dppf<X7>(t.x); t.y += dppf<X7>(t.y);
      if ((lane & 7) == 0) *(f32x2*)&lds[L_CS + ((w - 8) * 8 + (lane >> 3)) * 2] = t;
    }
    if (w < 10) {
      const int vb = WS_Y + ((xcd * 10 + w) * 32 + (lane & 7)) * 128 + gsel;
      int vo[4] = {vb, vb + 1024, vb + 2048, vb + 3072};
      f32x2 g[4];
      gather<4>(rs, vo, g, tag0, tag1);
      STAMP(11);
      float a0 = (g[0].x + g[1].x) + (g[2].x + g[3].x), a1 = (g[0].y + g[1].y) + (g[2].y + g[3].y);
      a0 += dppf<X1>(a0); a1 += dppf<X1>(a1);
      a0 += dppf<X2>(a0); a1 += dppf<X2>(a1);
      a0 += dppf<X7>(a0); a1 += dppf<X7>(a1);
      { const unsigned ab = cvtpk(a0, a1); a0 = bflo(ab); a1 = bfhi(ab); }
      const unsigned pk = cvtpk(bflo(xr) + a0, bfhi(xr) + a1);
      h0 = bflo(pk); h1 = bfhi(pk);
      const unsigned po = dppu<X8>(pk);
      const unsigned hb0 = (lane & 8) ? po : pk, hb1 = (lane & 8) ? pk : po;
      float v[16];
#pragma unroll
      for (int j = 0; j < 4; ++j) {
        v[2 * j] = dot2(gwt[0][j], hb0, 0.f); v[2 * j + 1] = dot2(gwt[1][j], hb0, 0.f);
        v[8 + 2 * j] = dot2(gwt[0][j], hb1, 0.f); v[8 + 2 * j + 1] = dot2(gwt[1][j], hb1, 0.f);
      }
      float st = tr32(h0 + h1, dot2(pk, pk, 0.f));
      float s8[8];
#pragma unroll
      for (int i = 0; i < 8; ++i) s8[i] = tr32(v[i], v[i + 8]);
      st = tr16(st, st);
      f32x4 s4;
#pragma unroll
      for (int i = 0; i < 4; ++i) s4[i] = tr16(s8[i], s8[i + 4]);
      *(f32x4*)&lds[L_PART + tid * 4] = s4;
      if ((lane & 23) == 0) lds[L_ST + w * 4 + ((lane >> 3) & 1) * 2 + (lane >> 5)] = st;
    }
    wg_barrier();
    if (tid < 130) {
      const int o = tid < 128 ? L_PART + tid * 2 : L_ST + (tid - 128) * 2, st = tid < 128 ? 256 : 4;
      f32x2 t = {0.f, 0.f};
#pragma unroll
      for (int ww = 0; ww < 10; ++ww) t += *(const f32x2*)&lds[o + ww * st];
      gst(rs, WS_GP + (idx * 8 + xcd) * GP_REC + tid * 16, __float_as_uint(t.x), __float_as_uint(t.y), tag0, tag1);
    }
    STAMP(12);
    if (w < 10) { wl = *(const u32x4*)&lds[L_WD + (w * 32 + (lane & 31)) * 4]; wh = *(const u32x4*)&lds[L_WD + 1280 + (w * 32 + (lane & 31)) * 4]; PIN(wl); PIN(wh); }
    {
      const int wl2 = (w + 3) & 15;
      const int vb = WS_GP + (idx * 8 + (lane & 7)) * GP_REC + (lane >> 3) * 16;
      const bool st2 = w == 5 && lane < 16;
      int vo[2] = {vb + wl2 * 128, st2 ? vb + 2048 : OOB};
      f32x2 g[2];
      gather<2>(rs, vo, g, tag0, tag1);
      f32x2 t = g[0], t2 = g[1];
      t.x += dppf<X1>(t.x); t.y += dppf<X1>(t.y); t2.x += dppf<X1>(t2.x); t2.y += dppf<X1>(t2.y);
      t.x += dppf<X2>(t.x); t.y += dppf<X2>(t.y); t2.x += dppf<X2>(t2.x); t2.y += dppf<X2>(t2.y);
      t.x += dppf<X7>(t.x); t.y += dppf<X7>(t.y); t2.x += dppf<X7>(t2.x); t2.y += dppf<X7>(t2.y);
      if ((lane & 7) == 0) {
        *(f32x2*)&lds[L_G2 + (wl2 * 8 + (lane >> 3)) * 2] = t;
        if (st2) *(f32x2*)&lds[L_G2 + (128 + (lane >> 3)) * 2] = t2;
      }
    }
    wg_barrier();
    STAMP(13);
    if (w < 10) {
      const int k8 = (lane >> 2) & 7, n2 = (lane >> 1) & 1, n1 = lane & 1, bb = lane >> 5;
      const int tg = bb * 64 + n2 * 32 + k8 * 2 + n1, tc = n2 * 32 + n1 * 16 + k8;
      const f32x2 g = *(const f32x2*)&lds[L_G2 + tg * 2], u = *(const f32x2*)&lds[L_G2 + (tg + 16) * 2];
      const f32x2 cg = *(const f32x2*)&lds[L_CS + tc * 2], cu = *(const f32x2*)&lds[L_CS + (tc + 8) * 2];
      const f32x2 st = *(const f32x2*)&lds[L_G2 + (128 + bb) * 2];
      const float mean = st.x * (1.f / D);
      const float var = fmaxf(fmaf(-mean, mean, st.y * (1.f / D)), 0.f);
      const float mb = bfr(mean), rstd = __builtin_amdgcn_rcpf(bfr(__builtin_amdgcn_sqrtf(bfr(bfr(var) + 1e-5f))));
      const unsigned gb = cvtpk(fmaf(-mb, cg.x, g.x) * rstd, fmaf(-mb, cg.y, g.y) * rstd);
      const unsigned ub = cvtpk(fmaf(-mb, cu.x, u.x) * rstd, fmaf(-mb, cu.y, u.y) * rstd);
      const unsigned hh = cvtpk(bflo(gb) * bflo(ub), bfhi(gb) * bfhi(ub));
      float v[8];
#pragma unroll
      for (int j = 0; j < 4; ++j) { v[2 * j] = dot2(wl[j], hh, 0.f); v[2 * j + 1] = dot2(wh[j], hh, 0.f); }
      float s4[4];
#pragma unroll
      for (int i = 0; i < 4; ++i) s4[i] = tr16(v[i], v[i + 4]);
      float t0 = trd<X8>(s4[0], s4[2], (c & 8) != 0), t1 = trd<X8>(s4[1], s4[3], (c & 8) != 0);
      t0 += dppf<X1>(t0); t1 += dppf<X1>(t1);
      t0 += dppf<X2>(t0); t1 += dppf<X2>(t1);
      t0 += dppf<X7>(t0); t1 += dppf<X7>(t1);
      if ((lane & 7) == 0) gst(rs, WS_OP + ((xcd * 10 + w) * 32 + idx) * 128 + lane * 2, __float_as_uint(t0), __float_as_uint(t1), tag0, tag1);
      STAMP(14);
      if (w == idx) {
        const int vb = WS_OP + ((xcd * 10 + w) * 32 + (lane & 7)) * 128 + gsel;
        int vo[4] = {vb, vb + 1024, vb + 2048, (lane & 7) < 3 ? vb + 3072 : OOB};
        f32x2 g4[4];
        gather<4>(rs, vo, g4, tag0, tag1);
        float a0 = (g4[0].x + g4[1].x) + (g4[2].x + g4[3].x), a1 = (g4[0].y + g4[1].y) + (g4[2].y + g4[3].y);
        a0 += dppf<X1>(a0); a1 += dppf<X1>(a1);
        a0 += dppf<X2>(a0); a1 += dppf<X2>(a1);
        a0 += dppf<X7>(a0); a1 += dppf<X7>(a1);
        { const unsigned ab = cvtpk(a0, a1); a0 = bflo(ab); a1 = bfhi(ab); }
        if ((lane & 7) == 0) *(GAS unsigned*)(out + ((lane >> 3) & 1) * (D * 2) + xcd * 160 + w * 16 + r * 4) = cvtpk(h0 + a0, h1 + a1);
        if (bid == 0 && lane == 0) {
          const u32x2 tg2 = {tag0, tag1};
          __builtin_amdgcn_raw_buffer_store_b64(tg2, rs, WS_EPOCH, 0, SC1);
        }
        STAMP(15);
      }
    }
  }
  STAMP_OUT();
}

__global__ void __launch_bounds__(1024) spin_stamps(const unsigned* st) {
  __shared__ unsigned mn, mx;
  if (threadIdx.x == 0) { mn = 0xffffffffu; mx = 0u; }
  __syncthreads();
  const unsigned ref = st[0] - 100000u;
  unsigned a = 0xffffffffu, b = 0u;
  for (int g = threadIdx.x; g < 4096; g += 1024) { a = min(a, st[g * NSTAMP] - ref); const unsigned t = st[g * NSTAMP + 0]; if (t) b = max(b, t - ref); }
  atomicMin(&mn, a); atomicMax(&mx, b);
  __syncthreads();
  if (threadIdx.x == 0) {
    unsigned d = (mx - mn) * 4; if (d > 100000u) d = 100000u;
    const unsigned long long t0 = __builtin_amdgcn_s_memrealtime();
    while (__builtin_amdgcn_s_memrealtime() - t0 < d) {}
  }
}
extern "C" void kernel_launch(void* const* d_in, const int* in_sizes, int n_in, void* d_out, int out_size, void* d_ws,
                              size_t ws_size, hipStream_t stream) {
  (void)in_sizes; (void)n_in; (void)out_size; (void)ws_size;
  fused_decode<<<NWG, NT, 0, stream>>>((const GAS char*)d_in[0], (const GAS char*)d_in[1], (const GAS char*)d_in[2],
                                       (const GAS char*)d_in[3], (const GAS char*)d_in[4], (const GAS char*)d_in[6],
                                       (const GAS char*)d_in[7], (const GAS char*)d_in[8], (GAS char*)d_out, (char*)d_ws);
  spin_stamps<<<1, 1024, 0, stream>>>((const unsigned*)((char*)d_ws + WS_STAMP));
}
```

```cpp
#include <hip/hip_runtime.h>
#include <stdint.h>

#define MIMIC 1

typedef unsigned u32x4 __attribute__((ext_vector_type(4)));
typedef unsigned u32x2 __attribute__((ext_vector_type(2)));
typedef float f32x4 __attribute__((ext_vector_type(4)));
typedef float f32x2 __attribute__((ext_vector_type(2)));
typedef __bf16 bf16x2 __attribute__((ext_vector_type(2)));
typedef __bf16 bf16x8 __attribute__((ext_vector_type(8)));
#define GAS __attribute__((address_space(1)))
#define LAS __attribute__((address_space(3)))
#define WAUX 2
#define PACE_US10 202
#define PACE_OFF 60
#define XQ 20
#define XO 14
#define QDELAY 40
#define QSLEEP 20
#define YA0 16
#define FT 0
#define D_OX 16
#define D_Y 20
#define D_GP 0
#define D_OP 24
#define KAUX 2
#define VAUX 2
#define ABL 0
#define PRIO 1

constexpr int D = 640, NH = 32, SK = 4096, DH = 128, FF = 1728;
constexpr int NWG = 256, NT = 1024;
constexpr unsigned SPIN_CAP = 1u << 22;

constexpr int WS_EPOCH = 0;
constexpr int WS_QX = 256;
constexpr int WS_ML = WS_QX + 256 * 2048;
constexpr int WS_OX = WS_ML + 4096;
constexpr int WS_Y = WS_OX + 256 * 1024;
constexpr int GP_REC = 17 * 128;
constexpr int WS_GP = WS_Y + 80 * 4096;
constexpr int WS_CS = WS_GP + 216 * GP_REC;
constexpr int WS_OP = WS_CS + 216 * 1024;
constexpr int WS_END = WS_OP + 80 * 4096;
static_assert(WS_OX % 128 == 0 && WS_Y % 128 == 0 && WS_GP % 128 == 0 && WS_OP % 128 == 0, "line alignment");

constexpr int L_PART = 0;
constexpr int L_QPK = L_PART + 2560;
constexpr int L_WML = L_QPK + 64;
constexpr int L_CTL = L_WML + 32;
constexpr int L_OA = L_CTL + 8;
constexpr int L_HA = L_OA + 128;
constexpr int L_HH = L_HA + 96;
constexpr int SO = 68, SG = 52, SD = 36;
constexpr int L_PO = L_HH + 64;
constexpr int L_PG = L_PO + 80 * SO;
constexpr int L_PD = L_PG + 128 * SG;
constexpr int L_END = 21504;
static_assert(L_PD + 80 * SD <= L_END, "LDS");

__device__ __forceinline__ float dot2(unsigned a, unsigned b, float c) {
  return __builtin_amdgcn_fdot2_f32_bf16(__builtin_bit_cast(bf16x2, a), __builtin_bit_cast(bf16x2, b), c, false);
}
__device__ __forceinline__ unsigned cvtpk(float lo, float hi) {
  bf16x2 v = {(__bf16)lo, (__bf16)hi};
  return __builtin_bit_cast(unsigned, v);
}
__device__ __forceinline__ float bflo(unsigned u) { return __builtin_bit_cast(float, u << 16); }
__device__ __forceinline__ float bfhi(unsigned u) { return __builtin_bit_cast(float, u & 0xffff0000u); }
__device__ __forceinline__ float bfr(float f) { return bflo(cvtpk(f, f)); }
__device__ __forceinline__ unsigned plo(unsigned a, unsigned b) { return __builtin_amdgcn_perm(b, a, 0x05040100u); }
__device__ __forceinline__ unsigned phi(unsigned a, unsigned b) { return __builtin_amdgcn_perm(b, a, 0x07060302u); }
template <int CTRL> __device__ __forceinline__ float dppf(float x) {
  return __builtin_bit_cast(float, __builtin_amdgcn_mov_dpp(__builtin_bit_cast(int, x), CTRL, 0xf, 0xf, true));
}
template <int CTRL> __device__ __forceinline__ unsigned dppu(unsigned x) {
  return (unsigned)__builtin_amdgcn_mov_dpp((int)x, CTRL, 0xf, 0xf, true);
}
constexpr int X1 = 0xB1, X2 = 0x4E, X7 = 0x141, X8 = 0x128, ROR4 = 0x124, ROR2 = 0x122, ROR1 = 0x121, BC = 0x150;
__device__ __forceinline__ float tr32(float a, float b) {
  auto s = __builtin_amdgcn_permlane32_swap(__float_as_uint(a), __float_as_uint(b), false, false);
  return __uint_as_float(s[0]) + __uint_as_float(s[1]);
}
__device__ __forceinline__ float tr16(float a, float b) {
  auto s = __builtin_amdgcn_permlane16_swap(__float_as_uint(a), __float_as_uint(b), false, false);
  return __uint_as_float(s[0]) + __uint_as_float(s[1]);
}
__device__ __forceinline__ float mx32(float a) {
  auto s = __builtin_amdgcn_permlane32_swap(__float_as_uint(a), __float_as_uint(a), false, false);
  return fmaxf(__uint_as_float(s[0]), __uint_as_float(s[1]));
}
__device__ __forceinline__ float mx16(float a) {
  auto s = __builtin_amdgcn_permlane16_swap(__float_as_uint(a), __float_as_uint(a), false, false);
  return fmaxf(__uint_as_float(s[0]), __uint_as_float(s[1]));
}
template <int CTRL> __device__ __forceinline__ float trd(float a, float b, bool sel) {
  const float t1 = a + dppf<CTRL>(a), t2 = b + dppf<CTRL>(b);
  return sel ? t2 : t1;
}
__device__ __forceinline__ float row_sum(float x) {
  x += dppf<X8>(x); x += dppf<ROR4>(x); x += dppf<X2>(x); x += dppf<X1>(x); return x;
}
__device__ __forceinline__ float row_max(float x) {
  x = fmaxf(x, dppf<X8>(x)); x = fmaxf(x, dppf<ROR4>(x)); x = fmaxf(x, dppf<X2>(x)); x = fmaxf(x, dppf<X1>(x)); return x;
}
__device__ __forceinline__ float wave_sum(float x) { x = row_sum(x); x = tr16(x, x); return tr32(x, x); }
__device__ __forceinline__ float wave_max(float x) { x = row_max(x); x = mx16(x); return mx32(x); }
__device__ __forceinline__ float rdl(float x, int l) { return __int_as_float(__builtin_amdgcn_readlane(__float_as_int(x), l)); }
__device__ __forceinline__ void wg_barrier() { asm volatile("s_waitcnt lgkmcnt(0)\n\ts_barrier" ::: "memory"); }
#define SB() __builtin_amdgcn_sched_barrier(0)
#define STAMP(i)
#define STAMP_DECL
#define STAMP_OUT()

constexpr int SC1 = 16, NTP = 2;
constexpr int OOB = 0x40000000;
typedef __amdgpu_buffer_rsrc_t rsrc_t;
__device__ __forceinline__ rsrc_t mk_rsrc(const GAS char* p, int bytes) {
  return __builtin_amdgcn_make_buffer_rsrc((void*)p, 0, bytes, 0x00020000);
}
template <int AUX> __device__ __forceinline__ u32x4 bld(rsrc_t r, int voff, int soff) {
  return __builtin_amdgcn_raw_buffer_load_b128(r, voff, soff, AUX);
}
#define PIN(x) asm volatile("" : "+v"(x))

__device__ __forceinline__ bool tagok(u32x4 v, unsigned t0, unsigned t1) { return v.y == t0 && v.w == t1; }
__device__ __forceinline__ u32x4 gld(rsrc_t rs, int voff, int soff) { return __builtin_amdgcn_raw_buffer_load_b128(rs, voff, soff, SC1); }
__device__ __forceinline__ void gst(rsrc_t rs, int voff, unsigned a, unsigned b, unsigned t0, unsigned t1) {
  const u32x4 g = {a, t0, b, t1};
  __builtin_amdgcn_raw_buffer_store_b128(g, rs, voff, 0, SC1);
}
#define FENCE() asm volatile("" ::: "memory")
#define PSLEEP 2
template <int N, int SLP = PSLEEP, int A0 = SC1>
__device__ __forceinline__ void gather(rsrc_t rs, int (&vo)[N], f32x2 (&v)[N], unsigned t0, unsigned t1) {
#pragma unroll
  for (int i = 0; i < N; ++i) { v[i].x = 0.f; v[i].y = 0.f; }
  u32x4 g[N];
  FENCE();
#pragma unroll
  for (int i = 0; i < N; ++i) g[i] = __builtin_amdgcn_raw_buffer_load_b128(rs, vo[i], 0, A0);
  for (unsigned spins = 0;; ++spins) {
    bool more = false;
#pragma unroll
    for (int i = 0; i < N; ++i) {
      const bool ok = tagok(g[i], t0, t1) && vo[i] != OOB;
      v[i].x = ok ? __uint_as_float(g[i].x) : v[i].x;
      v[i].y = ok ? __uint_as_float(g[i].z) : v[i].y;
      vo[i] = ok ? OOB : vo[i];
      more = more || vo[i] != OOB;
    }
    if (!__any(more) || spins > SPIN_CAP) break;
    if (SLP) __builtin_amdgcn_s_sleep(SLP);
    FENCE();
#pragma unroll
    for (int i = 0; i < N; ++i) g[i] = gld(rs, vo[i], 0);
  }
  FENCE();
}
__device__ __forceinline__ f32x4 mfma(u32x4 a, u32x4 b, f32x4 c) {
  return __builtin_amdgcn_mfma_f32_16x16x32_bf16(__builtin_bit_cast(bf16x8, a), __builtin_bit_cast(bf16x8, b), c, 0, 0, 0);
}
__device__ __forceinline__ void stash(unsigned* dst, int stride, u32x4 r0, u32x4 r1) {
#pragma unroll
  for (int j = 0; j < 4; ++j) { dst[(2 * j) * stride] = plo(r0[j], r1[j]); dst[(2 * j + 1) * stride] = phi(r0[j], r1[j]); }
}
__device__ __forceinline__ float sum8(f32x2 v) {
  float t = tr32(v.x, v.y);
  t += dppf<X8>(t);
  return tr16(t, t);
}

__global__ void __launch_bounds__(NT, 4)
fused_decode(const GAS char* __restrict__ x_, const GAS char* __restrict__ kc, const GAS char* __restrict__ vc,
             const GAS char* __restrict__ wq, const GAS char* __restrict__ wo, const GAS char* __restrict__ wg,
             const GAS char* __restrict__ wu, const GAS char* __restrict__ wd, GAS char* __restrict__ out, char* ws) {
  __shared__ __attribute__((aligned(16))) float lds[L_END];
  unsigned* const ldu = (unsigned*)lds;
  STAMP_DECL;
  STAMP(0);
  const unsigned tstart = (unsigned)__builtin_amdgcn_s_memrealtime();
  int tid = threadIdx.x, lane = tid & 63;
  const int w = __builtin_amdgcn_readfirstlane(tid >> 6);
  int r = lane >> 4, c = lane & 15;
#define REID() do { asm volatile("" : "+v"(tid)); lane = tid & 63; r = lane >> 4; c = lane & 15; } while (0)
  const int bid = blockIdx.x, xcd = bid & 7, idx = bid >> 3;
  const int head = xcd * 4 + (idx >> 3), b = (idx >> 2) & 1, s = idx & 3;
  const rsrc_t rs = __builtin_amdgcn_make_buffer_rsrc((void*)ws, 0, WS_END, 0x00020000);
  const bool roleG = idx < 27;
  const int sub = idx & 7, lid = head * 8 + sub;
  volatile LAS unsigned* ctl = (volatile LAS unsigned*)&lds[L_CTL];
  const int l16 = lane * 16;
  const size_t kvoff = ((size_t)((b * NH + head) * SK + s * 1024 + w * 64)) * 256;
  const rsrc_t rk = mk_rsrc(kc + kvoff, 16384), rv = mk_rsrc(vc + kvoff, 16384);
#define LDK(t) bld<KAUX>(rk, l16, (t) * 1024)
#define LDV(t) bld<VAUX>(rv, l16, (t) * 1024)
  constexpr unsigned PU = PACE_US10 * 10 * 16 / 32;
#define XTRA(n) (XQ + ((n) > 4 ? XO : 0) + ((n) > 8 ? XO : 0))
#define PACE(n) do { if (PU) { const unsigned tgt_ = tstart + PACE_OFF + (((((n) - 2) * 16 + XTRA(n) + ((w + 6) & 15)) * PU) >> 8); \
    while ((int)((unsigned)__builtin_amdgcn_s_memrealtime() - tgt_) < 0) __builtin_amdgcn_s_sleep(1); } SB(); } while (0)

  if (tid < 4) ctl[tid] = 0u;
  if (tid >= 64 && tid < 80) ldu[L_HA + ((tid >> 3) & 1) * 48 + 40 + (tid & 7)] = 0u;
  ldu[L_PG + (tid >> 3) * SG + 40 + (tid & 7)] = 0u;
  wg_barrier();
  u32x4 kr[16], vr[16], tw[4];
  unsigned xa[10], xb0, xb1;
  u32x4 wq0, wq1;
  u32x2 ep;
  if (w < 10) {
    const GAS unsigned* xdw = (const GAS unsigned*)x_;
    const int P = 40 * sub + 4 * w + r;
    const rsrc_t rq = mk_rsrc(wq + head * 256, (ABL & 1) ? 0 : D * 8192 - head * 256);
    wq0 = bld<WAUX>(rq, (2 * P) * 8192 + c * 16, 0);
    wq1 = bld<WAUX>(rq, (2 * P) * 8192 + c * 16, 8192);
    {
      const GAS unsigned* xh = xdw + (lane >> 5) * 320;
      const u32x4 t0 = *(const GAS u32x4*)(xh + 4 * (lane & 31)), t1 = *(const GAS u32x4*)(xh + 128 + 4 * (lane & 31));
      const u32x2 t2 = *(const GAS u32x2*)(xh + 256 + 2 * (lane & 31));
      xa[0] = t0.x; xa[1] = t0.y; xa[2] = t0.z; xa[3] = t0.w; xa[4] = t1.x; xa[5] = t1.y; xa[6] = t1.z; xa[7] = t1.w; xa[8] = t2.x; xa[9] = t2.y;
    }
    xb0 = xdw[P]; xb1 = xdw[320 + P];
    ep = __builtin_amdgcn_raw_buffer_load_b64(rs, w == 0 ? WS_EPOCH : OOB, 0, SC1);
  }
  SB();
  kr[0] = LDK(0); kr[1] = LDK(1);
  SB();
  STAMP(1);
  if (w < 10) {
    const unsigned ones = 0x3F803F80u;
    float sm = 0.f;
#pragma unroll
    for (int k = 0; k < 10; ++k) sm = dot2(xa[k], ones, sm);
    PIN(sm); SB();
    kr[2] = LDK(2);
    SB();
    sm = row_sum(sm); sm = tr16(sm, sm);
    const float mean = sm * (1.f / D);
    float sq = 0.f, sq2 = 0.f;
#pragma unroll
    for (int k = 0; k < 10; ++k) { const float a = bflo(xa[k]) - mean, bb = bfhi(xa[k]) - mean; sq = fmaf(a, a, sq); sq2 = fmaf(bb, bb, sq2); }
    sq += sq2;
    sq = row_sum(sq); sq = tr16(sq, sq);
    const float var = sq * (1.f / D);
    const float mb = bfr(mean), rstd = __builtin_amdgcn_rcpf(bfr(__builtin_amdgcn_sqrtf(bfr(bfr(var) + 1e-5f))));
    const float m0 = rdl(mb, 0), m1 = rdl(mb, 32), d0 = rdl(rstd, 0), d1 = rdl(rstd, 32);
    const unsigned xn0 = cvtpk(bfr(bflo(xb0) - m0) * d0, bfr(bfhi(xb0) - m0) * d0);
    const unsigned xn1 = cvtpk(bfr(bflo(xb1) - m1) * d1, bfr(bfhi(xb1) - m1) * d1);
    SB();
    kr[3] = LDK(3);
    SB();
    float a0[8], a1[8];
#pragma unroll
    for (int j = 0; j < 4; ++j) {
      const unsigned lo = plo(wq0[j], wq1[j]), hi = phi(wq0[j], wq1[j]);
      a0[2 * j] = dot2(lo, xn0, 0.f); a0[2 * j + 1] = dot2(hi, xn0, 0.f);
      a1[2 * j] = dot2(lo, xn1, 0.f); a1[2 * j + 1] = dot2(hi, xn1, 0.f);
    }
    float s0[4], s1[4];
#pragma unroll
    for (int i = 0; i < 4; ++i) { s0[i] = tr32(a0[i], a0[i + 4]); s1[i] = tr32(a1[i], a1[i + 4]); }
    f32x2 u0 = {tr16(s0[0], s0[2]), tr16(s0[1], s0[3])}, u1 = {tr16(s1[0], s1[2]), tr16(s1[1], s1[3])};
    *(f32x2*)&lds[L_PART + w * 256 + 8 * c + 2 * r] = u0;
    *(f32x2*)&lds[L_PART + w * 256 + 128 + 8 * c + 2 * r] = u1;
    FENCE();
    if (lane == 0) __hip_atomic_fetch_add((LAS unsigned*)&ctl[0], 1u, __ATOMIC_RELAXED, __HIP_MEMORY_SCOPE_WORKGROUP);
    STAMP(2);
    if (w == 0) {
      PIN(ep);
      const unsigned long long e64 = ((unsigned long long)ep.y << 32) | ep.x;
      const unsigned long long tg = e64 * 6364136223846793005ull + 1442695040888963407ull;
      const unsigned t0 = (unsigned)tg, t1 = (unsigned)(tg >> 32);
      if (lane == 0) { ctl[4] = t0; ctl[5] = t1; }
      while (ctl[0] < 10u) {}
      FENCE();
      f32x2 p0 = {0.f, 0.f}, p1 = {0.f, 0.f};
#pragma unroll
      for (int ww = 0; ww < 10; ++ww) {
        p0 += *(const f32x2*)&lds[L_PART + ww * 256 + 2 * lane];
        p1 += *(const f32x2*)&lds[L_PART + ww * 256 + 128 + 2 * lane];
      }
      gst(rs, WS_QX + lid * 2048 + l16, __float_as_uint(p0.x), __float_as_uint(p0.y), t0, t1);
      gst(rs, WS_QX + lid * 2048 + 1024 + l16, __float_as_uint(p1.x), __float_as_uint(p1.y), t0, t1);
      STAMP(3);
      int vo[8];
      f32x2 qq[8];
#pragma unroll
      for (int k = 0; k < 8; ++k) vo[k] = WS_QX + head * 16384 + b * 1024 + l16 + k * 2048;
      __builtin_amdgcn_s_sleep(QDELAY);
      gather<8, QSLEEP>(rs, vo, qq, t0, t1);
      const f32x2 qs = ((qq[0] + qq[1]) + (qq[2] + qq[3])) + ((qq[4] + qq[5]) + (qq[6] + qq[7]));
      ((unsigned*)lds)[L_QPK + lane] = cvtpk(qs.x, qs.y);
      FENCE();
      if (lane == 0) ctl[1] = 1u;
    }
  } else {
    PACE(2); kr[2] = LDK(2); SB();
    PACE(3); kr[3] = LDK(3); SB();
  }
  SB();
  REID();
  float sc;
  u32x4 qv;
  unsigned tag0, tag1;
  {
    const int Q = tid / 10, ch = tid - Q * 10;
    const rsrc_t rt = mk_rsrc(wo + (size_t)head * (128 * 1280) + sub * 160, ((ABL & 4) || w >= 10) ? 0 : 128 * 1280 - 1120);
    const int vo = (2 * Q) * 1280 + ch * 16;
    float l1[2], l2[2], l3[2], l4[2];
#define SCORE(t) do { const u32x4 kk = kr[t]; float d = dot2(kk[0], qv[0], 0.f); \
      d = dot2(kk[1], qv[1], d); d = dot2(kk[2], qv[2], d); d = dot2(kk[3], qv[3], d); l1[(t) & 1] = d; \
      if (((t) & 1) == 1) l2[((t) >> 1) & 1] = trd<X8>(l1[0], l1[1], (c & 8) != 0); \
      if (((t) & 3) == 3) l3[((t) >> 2) & 1] = trd<X7>(l2[0], l2[1], (c & 4) != 0); \
      if (((t) & 7) == 7) l4[((t) >> 3) & 1] = trd<X2>(l3[0], l3[1], (c & 2) != 0); SB(); } while (0)
#pragma unroll
    for (int n = 4; n < 22; ++n) {
      PACE(n);
      if (n < 16) kr[n] = LDK(n); else vr[n - 16] = LDV(n - 16);
      if (n == 4) tw[0] = bld<WAUX>(rt, vo, 0);
      if (n == 8) tw[1] = bld<WAUX>(rt, vo, 1280);
      SB();
      if (n == 13) { if (w < 10) stash(ldu + L_PO + (8 * ch) * SO + Q, SO, tw[0], tw[1]); SB(); }
      if (n == 14) {
        while (ctl[1] == 0u) __builtin_amdgcn_s_sleep(1);
        FENCE();
        qv = *(const u32x4*)&lds[L_QPK + 4 * c];
        tag0 = __builtin_amdgcn_readfirstlane(ctl[4]); tag1 = __builtin_amdgcn_readfirstlane(ctl[5]);
        STAMP(4);
      }
      if (n >= 14) { SCORE(2 * (n - 14)); SCORE(2 * (n - 14) + 1); }
    }
    sc = trd<X1>(l4[0], l4[1], (c & 1) != 0);
  }
  const float s2 = bfr(bfr(sc) * 0.08838834764831845f) * 1.4426950408889634f;
  unsigned ppk;
  {
    const float mw = wave_max(s2);
    const float p = __builtin_amdgcn_exp2f(s2 - mw);
    ppk = cvtpk(p, dppf<X8>(p));
    const float lw = wave_sum(p);
    unsigned old = 0u;
    if (lane == 0) {
      f32x2 t = {mw, lw}; *(f32x2*)&lds[L_WML + 2 * w] = t;
      FENCE();
      old = __hip_atomic_fetch_add((LAS unsigned*)&ctl[2], 1u, __ATOMIC_RELAXED, __HIP_MEMORY_SCOPE_WORKGROUP);
    }
    if (__builtin_amdgcn_readfirstlane(old) == 15u) {
      FENCE();
      const f32x2 ml = *(const f32x2*)&lds[L_WML + 2 * c];
      const float Mwg = row_max(ml.x);
      const float Lwg = row_sum(ml.y * __builtin_amdgcn_exp2f(ml.x - Mwg));
      if (lane == 0) gst(rs, WS_ML + ((b * NH + head) * 4 + s) * 16, __float_as_uint(Mwg), __float_as_uint(Lwg), tag0, tag1);
    }
  }
  STAMP(5);
  SB();
  REID();
  const int mloff = w == 0 ? WS_ML + ((b * NH + head) * 4 + (lane & 3)) * 16 : OOB;
  u32x4 mls;
  float wgt = 0.f;
  unsigned short xr;
  {
    float a[8];
#pragma unroll
    for (int i = 0; i < 8; ++i) a[i] = 0.f;
#define PV(u, L) do { const u32x4 v0 = vr[2 * (u)], v1 = vr[2 * (u) + 1]; const unsigned pp = dppu<BC + (L)>(ppk); \
      _Pragma("unroll") for (int j = 0; j < 4; ++j) { a[2 * j] = dot2(plo(v0[j], v1[j]), pp, a[2 * j]); a[2 * j + 1] = dot2(phi(v0[j], v1[j]), pp, a[2 * j + 1]); } \
      _Pragma("unroll") for (int i = 0; i < 8; ++i) PIN(a[i]); SB(); } while (0)
#pragma unroll
    for (int n = 22; n < 32; ++n) {
      PACE(n);
      vr[n - 16] = LDV(n - 16);
      if (n == 27) mls = gld(rs, mloff, 0);
      SB();
      if (n == 22) PV(0, 0);
      if (n == 23) PV(1, 4);
      if (n == 25) PV(2, 2);
      if (n == 27) PV(3, 6);
      if (n == 29) PV(4, 1);
      if (n == 31) PV(5, 5);
      if (n == 30 && w == 0) {
        for (unsigned spins = 0; !__all(tagok(mls, tag0, tag1)) && spins < SPIN_CAP; ++spins) {
          FENCE();
          mls = gld(rs, mloff, 0);
        }
        FENCE();
        const float ms = __uint_as_float(mls.x), ls = __uint_as_float(mls.z);
        float M = fmaxf(ms, dppf<X1>(ms)); M = fmaxf(M, dppf<X2>(M));
        float L = ls * __builtin_amdgcn_exp2f(ms - M);
        L += dppf<X1>(L); L += dppf<X2>(L);
        wgt = __builtin_amdgcn_exp2f(lds[L_WML + 2 * c] - M) / L;
        PIN(wgt);
        SB();
      }
    }
    REID();
#pragma unroll
    for (int e = 0; e < 2; ++e) {
      const int job = 2 * w + e, mat = job >= 5 ? 1 : 0;
      const bool isg = job < 10;
      const int tt = (job - 10) * 64 + lane, Q = tt / 10, ch = tt - Q * 10;
      const GAS char* base = isg ? (mat ? wu : wg) + (size_t)(80 * xcd + 16 * (job - 5 * mat)) * (FF * 2) + idx * 128
                                 : wd + (size_t)idx * (64 * 1280) + xcd * 160;
      const rsrc_t rt = mk_rsrc(base, (roleG && job < 15 && !(ABL & 2)) ? (isg ? 15 * FF * 2 + 128 : 63 * 1280 + 160) : 0);
      const int vo = isg ? (2 * (lane >> 3)) * (FF * 2) + (lane & 7) * 16 : (2 * Q) * 1280 + ch * 16;
      tw[2 * e] = bld<WAUX>(rt, vo, 0);
      tw[2 * e + 1] = bld<WAUX>(rt, vo, isg ? FF * 2 : 1280);
    }
    xr = *(const GAS unsigned short*)(x_ + (lane >> 5) * (D * 2) + xcd * 160 + ((w + 5) % 10) * 16 + (lane & 7) * 2);
    SB();
    PV(6, 3);
    PV(7, 7);
    float s4[4];
#pragma unroll
    for (int i = 0; i < 4; ++i) s4[i] = tr32(a[i], a[i + 4]);
    f32x2 u = {tr16(s4[0], s4[2]), tr16(s4[1], s4[3])};
    *(f32x2*)&lds[L_PART + w * 128 + 8 * c + 2 * r] = u;
  }
  STAMP(7);
  wg_barrier();
  if (w == 0) {
    f32x2 pr[16];
#pragma unroll
    for (int ww = 0; ww < 16; ++ww) pr[ww] = *(const f32x2*)&lds[L_PART + ww * 128 + 2 * lane];
    f32x2 o2 = {0.f, 0.f}, o3 = {0.f, 0.f};
#pragma unroll
    for (int ww = 0; ww < 16; ww += 2) { o2 += pr[ww] * rdl(wgt, ww); o3 += pr[ww + 1] * rdl(wgt, ww + 1); }
    o2 += o3;
    gst(rs, WS_OX + lid * 1024 + l16, __float_as_uint(o2.x), __float_as_uint(o2.y), tag0, tag1);
  }
  STAMP(8);
  u32x4 fb[4];
  if (w >= 8) {
    const int bb = (w >> 2) & 1, Q = (w & 3) * 16 + c;
    int vo[1] = {WS_OX + (head * 8 + bb * 4 + r) * 1024 + Q * 16};
    f32x2 g[1];
    __builtin_amdgcn_s_sleep(D_OX);
    gather<1>(rs, vo, g, tag0, tag1);
    float o = tr32(g[0].x, g[0].y);
    o = tr16(o, o);
    if ((lane & 16) == 0) ((unsigned short*)(ldu + L_OA))[bb * 128 + 2 * Q + (lane >> 5)] = (unsigned short)cvtpk(o, 0.f);
  } else {
#pragma unroll
    for (int e = 0; e < 2; ++e) {
      const int job = 2 * w + e, mat = job >= 5 ? 1 : 0;
      const int tt = (job - 10) * 64 + lane, Q = tt / 10, ch = tt - Q * 10;
      if (job < 10) stash(ldu + L_PG + (mat * 64 + 8 * (lane & 7)) * SG + 8 * (job - 5 * mat) + (lane >> 3), SG, tw[2 * e], tw[2 * e + 1]);
      else if (job < 15) stash(ldu + L_PD + (8 * ch) * SD + Q, SD, tw[2 * e], tw[2 * e + 1]);
    }
    if (w < 5) {
#pragma unroll
      for (int k = 0; k < 4; ++k) { fb[k] = *(const u32x4*)&ldu[L_PO + (16 * w + c) * SO + 16 * k + 4 * r]; PIN(fb[k]); }
    }
  }
  wg_barrier();
  STAMP(9);
  const f32x4 zero4 = {0.f, 0.f, 0.f, 0.f};
  if (w < 5) {
    u32x4 fa[4];
#pragma unroll
    for (int k = 0; k < 4; ++k) fa[k] = *(const u32x4*)&ldu[L_OA + (lane & 1) * 64 + 16 * k + 4 * r];
    f32x4 d0 = mfma(fa[0], fb[0], zero4), d1 = mfma(fa[1], fb[1], zero4);
    d0 = mfma(fa[2], fb[2], d0); d1 = mfma(fa[3], fb[3], d1);
    d0 += d1;
    if (lane < 16)
      gst(rs, WS_Y + ((sub * 10 + 2 * w + (lane >> 3)) * 32 + head) * 128 + (lane & 7) * 16, __float_as_uint(d0[0]), __float_as_uint(d0[1]), tag0, tag1);
  }
  STAMP(10);
  if (roleG) {
    const u32x4 ones4 = {0x3F803F80u, 0x3F803F80u, 0x3F803F80u, 0x3F803F80u};
    u32x4 fd[2];
    if (w < 8) {
#pragma unroll
      for (int k = 0; k < 3; ++k) { fb[k] = *(const u32x4*)&ldu[L_PG + (16 * w + c) * SG + 16 * k + 4 * r]; PIN(fb[k]); }
      if (w < 4) {
        f32x4 cg = zero4, cu = zero4;
#pragma unroll
        for (int k = 0; k < 3; ++k) {
          cg = mfma(ones4, fb[k], cg);
          cu = mfma(ones4, *(const u32x4*)&ldu[L_PG + (64 + 16 * w + c) * SG + 16 * k + 4 * r], cu);
        }
        if (lane < 16) gst(rs, WS_CS + (idx * 8 + xcd) * 1024 + w * 256 + l16, __float_as_uint(cg[0]), __float_as_uint(cu[0]), tag0, tag1);
      }
      if (w < 5) {
#pragma unroll
        for (int k = 0; k < 2; ++k) { fd[k] = *(const u32x4*)&ldu[L_PD + (16 * w + c) * SD + 16 * k + 4 * r]; PIN(fd[k]); }
      }
    }
    float hres = 0.f;
    const int j10 = w - 5;
    if (w >= 5 && w < 15) {
      const int vb = WS_Y + (xcd * 10 + j10) * 4096 + lane * 16;
      int vo[4] = {vb, vb + 1024, vb + 2048, vb + 3072};
      f32x2 g[4];
      __builtin_amdgcn_s_sleep(D_Y);
      gather<4, PSLEEP, YA0>(rs, vo, g, tag0, tag1);
      STAMP(11);
      float at = sum8((g[0] + g[1]) + (g[2] + g[3]));
      at = bfr(at);
      const unsigned pk = cvtpk(__uint_as_float((unsigned)xr << 16) + at, 0.f);
      hres = bflo(pk);
      if ((lane & 24) == 0) ((unsigned short*)(ldu + L_HA))[(lane >> 5) * 96 + 8 * j10 + (lane & 7)] = (unsigned short)pk;
    }
    wg_barrier();
    if (w < 8 || w == 15) {
      u32x4 fa[3];
#pragma unroll
      for (int k = 0; k < 3; ++k) fa[k] = *(const u32x4*)&ldu[L_HA + (lane & 1) * 48 + 16 * k + 4 * r];
      if (w == 15) {
#pragma unroll
        for (int k = 0; k < 3; ++k) fb[k] = c == 2 ? ones4 : fa[k];
      }
      f32x4 d0 = mfma(fa[0], fb[0], zero4), d1 = mfma(fa[1], fb[1], zero4);
      d0 = mfma(fa[2], fb[2], d0);
      d0 += d1;
      if (lane < (w == 15 ? 3 : 16))
        gst(rs, WS_GP + (idx * 8 + xcd) * GP_REC + (w == 15 ? 8 : w) * 256 + l16, __float_as_uint(d0[0]), __float_as_uint(d0[1]), tag0, tag1);
    }
    STAMP(12);
    if (w >= 8) {
      const int w8 = w - 8;
      float cg, cu;
      {
        int vo[1] = {WS_CS + (idx * 8 + (lane >> 3)) * 1024 + w8 * 128 + (lane & 7) * 16};
        f32x2 g[1];
        gather<1>(rs, vo, g, tag0, tag1);
        const float t = sum8(g[0]);
        const auto sw = __builtin_amdgcn_permlane32_swap(__float_as_uint(t), __float_as_uint(t), false, false);
        cg = __uint_as_float(sw[0]); cu = __uint_as_float(sw[1]);
        PIN(cg); PIN(cu);
      }
      const int vb = WS_GP + (idx * 8 + (lane >> 3)) * GP_REC + (lane & 7) * 16;
      int vo[3] = {vb + w8 * 128, vb + 1024 + w8 * 128, (lane & 7) < 3 ? vb + 2048 : OOB};
      f32x2 g[3];
      __builtin_amdgcn_s_sleep(D_GP);
      gather<3>(rs, vo, g, tag0, tag1);
      const float gs = sum8(g[0]), us = sum8(g[1]), st = sum8(g[2]);
      const bool b1 = lane >= 32;
      const float s2a = rdl(st, 0), s2b = rdl(st, 33), s1a = rdl(st, 2), s1b = rdl(st, 34);
      const float mean = (b1 ? s1b : s1a) * (1.f / D);
      const float var = fmaxf(fmaf(-mean, mean, (b1 ? s2b : s2a) * (1.f / D)), 0.f);
      const float mb = bfr(mean), rstd = __builtin_amdgcn_rcpf(bfr(__builtin_amdgcn_sqrtf(bfr(bfr(var) + 1e-5f))));
      const unsigned gu = cvtpk(fmaf(-mb, cg, gs) * rstd, fmaf(-mb, cu, us) * rstd);
      const unsigned hh = cvtpk(bflo(gu) * bfhi(gu), 0.f);
      if ((lane & 24) == 0) ((unsigned short*)(ldu + L_HH))[(lane >> 5) * 64 + 8 * w8 + (lane & 7)] = (unsigned short)hh;
    }
    wg_barrier();
    STAMP(13);
    if (w < 5) {
      const u32x4 fa0 = *(const u32x4*)&ldu[L_HH + (lane & 1) * 32 + 4 * r], fa1 = *(const u32x4*)&ldu[L_HH + (lane & 1) * 32 + 16 + 4 * r];
      const f32x4 d0 = mfma(fa0, fd[0], zero4) + mfma(fa1, fd[1], zero4);
      if (lane < 16)
        gst(rs, WS_OP + ((xcd * 10 + 2 * w + (lane >> 3)) * 32 + idx) * 128 + (lane & 7) * 16, __float_as_uint(d0[0]), __float_as_uint(d0[1]), tag0, tag1);
      STAMP(14);
    }
    if (j10 == idx && idx < 10) {
      const int vb = WS_OP + (xcd * 10 + j10) * 4096 + lane * 16;
      int vo[4] = {vb, vb + 1024, vb + 2048, lane < 24 ? vb + 3072 : OOB};
      f32x2 g[4];
      __builtin_amdgcn_s_sleep(D_OP);
      gather<4>(rs, vo, g, tag0, tag1);
      float a = sum8((g[0] + g[1]) + (g[2] + g[3]));
      a = bfr(a);
      if ((lane & 24) == 0)
        *(GAS unsigned short*)(out + (lane >> 5) * (D * 2) + xcd * 160 + j10 * 16 + (lane & 7) * 2) = (unsigned short)cvtpk(hres + a, 0.f);
      if (bid == 0 && lane == 0) {
        const u32x2 tg2 = {tag0, tag1};
        __builtin_amdgcn_raw_buffer_store_b64(tg2, rs, WS_EPOCH, 0, SC1);
      }
      STAMP(15);
    }
  }
  STAMP_OUT();
}

extern "C" void kernel_launch(void* const* d_in, const int* in_sizes, int n_in, void* d_out, int out_size, void* d_ws,
                              size_t ws_size, hipStream_t stream) {
  (void)in_sizes; (void)n_in; (void)out_size; (void)ws_size;
  fused_decode<<<NWG, NT, 0, stream>>>((const GAS char*)d_in[0], (const GAS char*)d_in[1], (const GAS char*)d_in[2],
                                       (const GAS char*)d_in[3], (const GAS char*)d_in[4], (const GAS char*)d_in[6],
                                       (const GAS char*)d_in[7], (const GAS char*)d_in[8], (GAS char*)d_out, (char*)d_ws);
}
```

```cpp
#include <hip/hip_runtime.h>
#include <stdint.h>
#include <utility>
#include <type_traits>

typedef unsigned u32x4 __attribute__((ext_vector_type(4)));
typedef unsigned u32x2 __attribute__((ext_vector_type(2)));
typedef float f32x4 __attribute__((ext_vector_type(4)));
typedef float f32x2 __attribute__((ext_vector_type(2)));
typedef __bf16 bf16x2 __attribute__((ext_vector_type(2)));
typedef __bf16 bf16x8 __attribute__((ext_vector_type(8)));
#define GAS __attribute__((address_space(1)))
#define LAS __attribute__((address_space(3)))

constexpr int D = 640, NH = 32, SK = 4096, FF = 1728;
constexpr int NWG = 256, NT = 1024;
constexpr unsigned SPIN_CAP = 1u << 19;

constexpr unsigned PACE_US10 = 190;
constexpr unsigned PACE_OFF = 60;
constexpr int XS = 4, XQ = 10, XD = 4;
constexpr int XE = 1, XEN = 25;
constexpr int WO_LATE = 14;
constexpr int CATCH = 6;
constexpr int WIN = 3;
constexpr int LNW = 10, LNS = 4;
constexpr int PTN = 13, PTS = 16;
constexpr int D_OX = 20, D_Y = 20, D_GP = 22, D_ST = 24, D_OP = 24;

constexpr int WS_EPOCH = 0;
constexpr int WS_QX = 256;
constexpr int WS_ML = WS_QX + 256 * 1024;
constexpr int WS_OX = WS_ML + 4096;
constexpr int WS_Y = WS_OX + 256 * 1024;
constexpr int GP_REC = 17 * 128;
constexpr int WS_GP = WS_Y + 80 * 4096;
constexpr int WS_CS = WS_GP + 216 * GP_REC;
constexpr int WS_OP = WS_CS + 216 * 1024;
constexpr int WS_END = WS_OP + 80 * 4096;
static_assert(WS_OX % 128 == 0 && WS_Y % 128 == 0 && WS_GP % 128 == 0 && WS_OP % 128 == 0, "line alignment");

constexpr int L_PART = 0;
constexpr int L_QPK = L_PART + 2560;
constexpr int L_WML = L_QPK + 64;
constexpr int L_CTL = L_WML + 32;
constexpr int L_ST = L_CTL + 12;
constexpr int L_XN = L_ST + 20;
constexpr int L_OA = L_XN + 160;
constexpr int L_HA = L_OA + 128;
constexpr int L_HH = L_HA + 96;
constexpr int SO = 68, SG = 52, SD = 36;
constexpr int L_PO = L_HH + 64;
constexpr int L_PG = L_PO + 80 * SO;
constexpr int L_PD = L_PG + 128 * SG;
constexpr int L_END = 21504;
static_assert(L_PD + 80 * SD <= L_END, "LDS");
static_assert(LNS >= 3, "the control words are zeroed by the barrier of step 3");

__device__ __forceinline__ float dot2(unsigned a, unsigned b, float c) {
  return __builtin_amdgcn_fdot2_f32_bf16(__builtin_bit_cast(bf16x2, a), __builtin_bit_cast(bf16x2, b), c, false);
}
__device__ __forceinline__ unsigned cvtpk(float lo, float hi) {
  bf16x2 v = {(__bf16)lo, (__bf16)hi};
  return __builtin_bit_cast(unsigned, v);
}
__device__ __forceinline__ float bflo(unsigned u) { return __builtin_bit_cast(float, u << 16); }
__device__ __forceinline__ float bfhi(unsigned u) { return __builtin_bit_cast(float, u & 0xffff0000u); }
__device__ __forceinline__ float bfr(float f) { return bflo(cvtpk(f, f)); }
__device__ __forceinline__ unsigned plo(unsigned a, unsigned b) { return __builtin_amdgcn_perm(b, a, 0x05040100u); }
__device__ __forceinline__ unsigned phi(unsigned a, unsigned b) { return __builtin_amdgcn_perm(b, a, 0x07060302u); }
template <int CTRL> __device__ __forceinline__ float dppf(float x) {
  return __builtin_bit_cast(float, __builtin_amdgcn_mov_dpp(__builtin_bit_cast(int, x), CTRL, 0xf, 0xf, true));
}
template <int CTRL> __device__ __forceinline__ unsigned dppu(unsigned x) {
  return (unsigned)__builtin_amdgcn_mov_dpp((int)x, CTRL, 0xf, 0xf, true);
}
constexpr int X1 = 0xB1, X2 = 0x4E, X7 = 0x141, X8 = 0x128, ROR4 = 0x124, ROR2 = 0x122, ROR1 = 0x121, BC = 0x150;
__device__ __forceinline__ float tr32(float a, float b) {
  auto s = __builtin_amdgcn_permlane32_swap(__float_as_uint(a), __float_as_uint(b), false, false);
  return __uint_as_float(s[0]) + __uint_as_float(s[1]);
}
__device__ __forceinline__ float tr16(float a, float b) {
  auto s = __builtin_amdgcn_permlane16_swap(__float_as_uint(a), __float_as_uint(b), false, false);
  return __uint_as_float(s[0]) + __uint_as_float(s[1]);
}
__device__ __forceinline__ float mx32(float a) {
  auto s = __builtin_amdgcn_permlane32_swap(__float_as_uint(a), __float_as_uint(a), false, false);
  return fmaxf(__uint_as_float(s[0]), __uint_as_float(s[1]));
}
__device__ __forceinline__ float mx16(float a) {
  auto s = __builtin_amdgcn_permlane16_swap(__float_as_uint(a), __float_as_uint(a), false, false);
  return fmaxf(__uint_as_float(s[0]), __uint_as_float(s[1]));
}
template <int CTRL> __device__ __forceinline__ float trd(float a, float b, bool sel) {
  const float t1 = a + dppf<CTRL>(a), t2 = b + dppf<CTRL>(b);
  return sel ? t2 : t1;
}
__device__ __forceinline__ float row_sum(float x) {
  x += dppf<X8>(x); x += dppf<ROR4>(x); x += dppf<X2>(x); x += dppf<X1>(x); return x;
}
__device__ __forceinline__ float row_max(float x) {
  x = fmaxf(x, dppf<X8>(x)); x = fmaxf(x, dppf<ROR4>(x)); x = fmaxf(x, dppf<X2>(x)); x = fmaxf(x, dppf<X1>(x)); return x;
}
__device__ __forceinline__ float wave_sum(float x) { x = row_sum(x); x = tr16(x, x); return tr32(x, x); }
__device__ __forceinline__ float wave_max(float x) { x = row_max(x); x = mx16(x); return mx32(x); }
__device__ __forceinline__ float rdl(float x, int l) { return __int_as_float(__builtin_amdgcn_readlane(__float_as_int(x), l)); }
__device__ __forceinline__ void wg_barrier() { asm volatile("s_waitcnt lgkmcnt(0)\n\ts_barrier" ::: "memory"); }
#define SB() __builtin_amdgcn_sched_barrier(0)

constexpr int SC1 = 16, NTP = 2;
constexpr int OOB = 0x40000000;
typedef __amdgpu_buffer_rsrc_t rsrc_t;
__device__ __forceinline__ rsrc_t mk_rsrc(const GAS char* p, int bytes) {
  return __builtin_amdgcn_make_buffer_rsrc((void*)p, 0, bytes, 0x00020000);
}
template <int AUX> __device__ __forceinline__ u32x4 bld(rsrc_t r, int voff, int soff) {
  return __builtin_amdgcn_raw_buffer_load_b128(r, voff, soff, AUX);
}
#define PIN(x) asm volatile("" : "+v"(x))

__device__ __forceinline__ bool tagok(u32x4 v, unsigned t0, unsigned t1) { return v.y == t0 && v.w == t1; }
__device__ __forceinline__ u32x4 gld(rsrc_t rs, int voff, int soff) { return __builtin_amdgcn_raw_buffer_load_b128(rs, voff, soff, SC1); }
__device__ __forceinline__ void gst(rsrc_t rs, int voff, unsigned a, unsigned b, unsigned t0, unsigned t1) {
  const u32x4 g = {a, t0, b, t1};
  __builtin_amdgcn_raw_buffer_store_b128(g, rs, voff, 0, SC1);
}
#define FENCE() asm volatile("" ::: "memory")
template <int N>
__device__ __forceinline__ bool accept(const u32x4 (&g)[N], int (&vo)[N], f32x2 (&v)[N], unsigned t0, unsigned t1) {
  bool more = false;
#pragma unroll
  for (int i = 0; i < N; ++i) {
    const bool ok = tagok(g[i], t0, t1) && vo[i] != OOB;
    v[i].x = ok ? __uint_as_float(g[i].x) : v[i].x;
    v[i].y = ok ? __uint_as_float(g[i].z) : v[i].y;
    vo[i] = ok ? OOB : vo[i];
    more = more || vo[i] != OOB;
  }
  return !__any(more);
}
template <int N> __device__ __forceinline__ void sweep(rsrc_t rs, const int (&vo)[N], u32x4 (&g)[N]) {
  FENCE();
#pragma unroll
  for (int i = 0; i < N; ++i) g[i] = gld(rs, vo[i], 0);
  FENCE();
}
template <int N>
__device__ __forceinline__ void gather(rsrc_t rs, int (&vo)[N], f32x2 (&v)[N], unsigned t0, unsigned t1) {
#pragma unroll
  for (int i = 0; i < N; ++i) { v[i].x = 0.f; v[i].y = 0.f; }
  u32x4 g[N];
  sweep<N>(rs, vo, g);
#pragma nounroll
  for (unsigned spins = 0; spins < SPIN_CAP; ++spins) {
    if (accept<N>(g, vo, v, t0, t1)) break;
    __builtin_amdgcn_s_sleep(2);
    sweep<N>(rs, vo, g);
  }
  FENCE();
}
template <int B, class F, int... I> __device__ __forceinline__ void steps(F& f, std::integer_sequence<int, I...>) {
  (f(std::integral_constant<int, B + I>{}), ...);
}
__device__ __forceinline__ f32x4 mfma(u32x4 a, u32x4 b, f32x4 c) {
  return __builtin_amdgcn_mfma_f32_16x16x32_bf16(__builtin_bit_cast(bf16x8, a), __builtin_bit_cast(bf16x8, b), c, 0, 0, 0);
}
__device__ __forceinline__ void stash(unsigned* dst, int stride, u32x4 r0, u32x4 r1) {
#pragma unroll
  for (int j = 0; j < 4; ++j) { dst[(2 * j) * stride] = plo(r0[j], r1[j]); dst[(2 * j + 1) * stride] = phi(r0[j], r1[j]); }
}
__device__ __forceinline__ float sum8(f32x2 v) {
  float t = tr32(v.x, v.y);
  t += dppf<X8>(t);
  return tr16(t, t);
}

__global__ void __launch_bounds__(NT, 4)
fused_decode(const GAS char* __restrict__ x_, const GAS char* __restrict__ kc, const GAS char* __restrict__ vc,
             const GAS char* __restrict__ wq, const GAS char* __restrict__ wo, const GAS char* __restrict__ wg,
             const GAS char* __restrict__ wu, const GAS char* __restrict__ wd, GAS char* __restrict__ out, char* ws) {
  __shared__ __attribute__((aligned(16))) float lds[L_END];
  unsigned* const ldu = (unsigned*)lds;
  const unsigned tstart = (unsigned)__builtin_amdgcn_s_memrealtime();
  SB();
  int tid = threadIdx.x, lane = tid & 63;
  const int w = __builtin_amdgcn_readfirstlane(tid >> 6);
  int r = lane >> 4, c = lane & 15;
#define REID() do { asm volatile("" : "+v"(tid)); lane = tid & 63; r = lane >> 4; c = lane & 15; } while (0)
  const int bid = blockIdx.x, xcd = bid & 7, idx = bid >> 3;
  const int head = xcd * 4 + (idx >> 3), b = (idx >> 2) & 1, s = idx & 3;
  const rsrc_t rs = __builtin_amdgcn_make_buffer_rsrc((void*)ws, 0, WS_END, 0x00020000);
  const int sub = idx & 7, lid = head * 8 + sub;
  const int fcb = head, frb = sub;
  const bool roleG = fcb < 27;
  volatile LAS unsigned* ctl = (volatile LAS unsigned*)&lds[L_CTL];
  const int l16 = lane * 16;
  const size_t kvoff = ((size_t)((b * NH + head) * SK + s * 1024 + w * 4)) * 256;
  const rsrc_t rk = mk_rsrc(kc + kvoff, 15 * 16384 + 1024), rv = mk_rsrc(vc + kvoff, 15 * 16384 + 1024);
#define LDK(t) bld<NTP>(rk, l16, (t) * 16384)
#define LDV(t) bld<NTP>(rv, l16, (t) * 16384)
  constexpr unsigned PU = PACE_US10 * 10 * 16 / 32;
#define XTRA(n) (XS + ((n) > 1 ? XQ : 0) + ((n) > 2 ? XQ : 0) + ((n) > 23 ? XD : 0) + ((n) > 24 ? XD : 0) - ((n) > XEN ? ((n) - XEN) * XE : 0))
#define SLOT(n) (PACE_OFF + (((((n) - 1) * 16 + XTRA(n)) * PU) >> 8))
#define WAIT_UNTIL(t) do { const unsigned tgt_ = (t); \
    while ((int)((unsigned)__builtin_amdgcn_s_memrealtime() - tgt_) < 0) __builtin_amdgcn_s_sleep(1); } while (0)
  int slip = 0;
#define PACE(n) do { WAIT_UNTIL(tstart + slip + SLOT(n)); slip = slip > CATCH ? slip - CATCH : 0; \
    if ((n) > 5 && ((n) < 13 || (n) > 15) && ((n) < 24 || (n) > 26)) asm volatile("s_waitcnt vmcnt(%0)" :: "i"(WIN) : "memory"); \
    SB(); } while (0)

  u32x4 kr[16], vr[16], tw[4];
  kr[0] = LDK(0);
  SB();
  float sc;
  u32x4 qv;
  unsigned tag0, tag1;
  {
    const rsrc_t rx = mk_rsrc(x_, 2 * D * 2);
    const rsrc_t rq = mk_rsrc(wq + head * 256 + (sub & 1) * 128, w >= 10 ? 0 : (D - 1) * 8192 + 128);
    const int P = 80 * (sub >> 1) + 8 * w + (lane >> 3);
    const int xo = (lane >> 5) * (D * 2) + (lane & 31) * 16;
    u32x4 xt0, xt1, xb;
    u32x2 xt2, ep;
    if (w == LNW) {
      xt0 = bld<0>(rx, xo, 0); xt1 = bld<0>(rx, xo, 512);
      xt2 = __builtin_amdgcn_raw_buffer_load_b64(rx, (lane >> 5) * (D * 2) + (lane & 31) * 8, 1024, 0);
      xb = bld<0>(rx, (lane & 31) < 20 ? (lane >> 5) * (D * 2) + (sub >> 1) * 320 + (lane & 31) * 16 : OOB, 0);
      ep = __builtin_amdgcn_raw_buffer_load_b64(rs, WS_EPOCH, 0, SC1);
    }
    SB();
    if (tid < 12) ctl[tid] = 0u;
    if (tid >= 64 && tid < 80) ldu[L_HA + ((tid >> 3) & 1) * 48 + 40 + (tid & 7)] = 0u;
    ldu[L_PG + (tid >> 3) * SG + 40 + (tid & 7)] = 0u;
    u32x4 wq0, wq1, qg[4];
    unsigned ptv = 0u;
    const int qoff = w == 0 ? WS_QX + (head * 8 + (lane >> 5)) * 1024 + b * 512 + (lane & 31) * 16 : OOB;
    float l1[2], l2[2], l3[2], l4[2];
#define SCORE(t) do { const u32x4 kk = kr[t]; float d = dot2(kk[0], qv[0], 0.f); \
      d = dot2(kk[1], qv[1], d); d = dot2(kk[2], qv[2], d); d = dot2(kk[3], qv[3], d); l1[(t) & 1] = d; \
      if (((t) & 1) == 1) l2[((t) >> 1) & 1] = trd<X8>(l1[0], l1[1], (c & 8) != 0); \
      if (((t) & 3) == 3) l3[((t) >> 2) & 1] = trd<X7>(l2[0], l2[1], (c & 4) != 0); \
      if (((t) & 7) == 7) l4[((t) >> 3) & 1] = trd<X2>(l3[0], l3[1], (c & 2) != 0); SB(); } while (0)
    auto kstep = [&](auto N_) __attribute__((always_inline)) {
      constexpr int n = decltype(N_)::value;
      if constexpr (n == 3) {
        PIN(kr[1]);
        wg_barrier();
        const int late = (int)((unsigned)__builtin_amdgcn_s_memrealtime() - tstart - SLOT(n));
        if (late > 0) slip = late;
      }
      PACE(n);
      if constexpr (n < 16) kr[n] = LDK(n); else vr[n - 16] = LDV(n - 16);
      if (n == 1) wq0 = bld<NTP>(rq, (2 * P) * 8192 + (lane & 7) * 16, 0);
      if (n == 2) wq1 = bld<NTP>(rq, (2 * P) * 8192 + (lane & 7) * 16, 8192);
      if (n == PTN && w == 13) {
        const int fc = roleG ? fcb : 0, k = lane & 15, g = lane >> 4;
        const int ro = min(k * PTS, 127) * 1280, rg = min(k * (PTS * 1280 / 3456 + 1), 79) * (FF * 2);
        const GAS char* tp = g == 0 ? wo + (size_t)head * (128 * 1280) + sub * 160 + ro
                           : (g == 1 ? wg : wu) + (size_t)(80 * frb) * (FF * 2) + fc * 128 + rg;
        if (g < 3) ptv = __builtin_nontemporal_load((const GAS unsigned*)tp);
      }
      if (n == PTN + 3 && w == 13) asm volatile("" :: "v"(ptv));
      if (n == 12 && w == 0) { qg[0] = gld(rs, qoff, 0); qg[1] = gld(rs, qoff, 2048); qg[2] = gld(rs, qoff, 4096); qg[3] = gld(rs, qoff, 6144); }
      SB();
      if (n == LNS && w == LNW) {
        const unsigned xa[10] = {xt0.x, xt0.y, xt0.z, xt0.w, xt1.x, xt1.y, xt1.z, xt1.w, xt2.x, xt2.y};
        const unsigned ones = 0x3F803F80u;
        float sm = 0.f;
#pragma unroll
        for (int k = 0; k < 10; ++k) sm = dot2(xa[k], ones, sm);
        sm = row_sum(sm); sm = tr16(sm, sm);
        const float mean = sm * (1.f / D);
        float sq = 0.f, sq2 = 0.f;
#pragma unroll
        for (int k = 0; k < 10; ++k) { const float a = bflo(xa[k]) - mean, bb = bfhi(xa[k]) - mean; sq = fmaf(a, a, sq); sq2 = fmaf(bb, bb, sq2); }
        sq += sq2;
        sq = row_sum(sq); sq = tr16(sq, sq);
        const float var = sq * (1.f / D);
        u32x4 xn;
        const float mb = bfr(mean), rstd = __builtin_amdgcn_rcpf(bfr(__builtin_amdgcn_sqrtf(bfr(bfr(var) + 1e-5f))));
#pragma unroll
        for (int k = 0; k < 4; ++k) xn[k] = cvtpk(bfr(bflo(xb[k]) - mb) * rstd, bfr(bfhi(xb[k]) - mb) * rstd);
        if ((lane & 31) < 20) *(u32x4*)&ldu[L_XN + (lane >> 5) * 80 + (lane & 31) * 4] = xn;
        PIN(ep);
        const unsigned long long e64 = ((unsigned long long)ep.y << 32) | ep.x;
        const unsigned long long tg = e64 * 6364136223846793005ull + 1442695040888963407ull;
        if (lane == 0) { ctl[4] = (unsigned)tg; ctl[5] = (unsigned)(tg >> 32); }
        FENCE();
        if (lane == 0) ctl[3] = 1u;
        SB();
      }
      if (n == 6 && w < 10) {
        while (ctl[3] == 0u) __builtin_amdgcn_s_sleep(1);
        FENCE();
        const unsigned xn0 = ldu[L_XN + 8 * w + (lane >> 3)], xn1 = ldu[L_XN + 80 + 8 * w + (lane >> 3)];
        float v[16];
#pragma unroll
        for (int j = 0; j < 4; ++j) {
          const unsigned lo = plo(wq0[j], wq1[j]), hi = phi(wq0[j], wq1[j]);
          v[2 * j] = dot2(lo, xn0, 0.f); v[2 * j + 1] = dot2(hi, xn0, 0.f);
          v[8 + 2 * j] = dot2(lo, xn1, 0.f); v[8 + 2 * j + 1] = dot2(hi, xn1, 0.f);
        }
        float s8[8], s4[4];
#pragma unroll
        for (int i = 0; i < 8; ++i) s8[i] = tr32(v[i], v[i + 8]);
#pragma unroll
        for (int i = 0; i < 4; ++i) s4[i] = tr16(s8[i], s8[i + 4]);
        const f32x2 t = {trd<X8>(s4[0], s4[2], (lane & 8) != 0), trd<X8>(s4[1], s4[3], (lane & 8) != 0)};
        {
          *(f32x2*)&lds[L_PART + w * 128 + ((lane >> 5) * 32 + (lane & 7) * 4 + ((lane >> 3) & 3)) * 2] = t;
          FENCE();
          if (lane == 0) __hip_atomic_fetch_add((LAS unsigned*)&ctl[0], 1u, __ATOMIC_RELAXED, __HIP_MEMORY_SCOPE_WORKGROUP);
        }
        SB();
      }
      if (n >= 7 && n <= 10 && w == 11 && n == 7 + (head & 3)) {
        while (ctl[0] < 10u) __builtin_amdgcn_s_sleep(1);
        FENCE();
        f32x2 q2 = {0.f, 0.f};
#pragma unroll
        for (int ww = 0; ww < 10; ++ww) q2 += *(const f32x2*)&lds[L_PART + ww * 128 + lane * 2];
        gst(rs, WS_QX + lid * 1024 + l16, __float_as_uint(q2.x), __float_as_uint(q2.y), ctl[4], ctl[5]);
        SB();
      }
      if (n == 14 && w == 0) {
        const unsigned t0 = ctl[4], t1 = ctl[5];
        int vo[4] = {qoff, qoff + 2048, qoff + 4096, qoff + 6144};
        f32x2 qq[4] = {{0.f, 0.f}, {0.f, 0.f}, {0.f, 0.f}, {0.f, 0.f}};
#pragma nounroll
        for (unsigned spins = 0; !accept<4>(qg, vo, qq, t0, t1) && spins < SPIN_CAP; ++spins) {
          __builtin_amdgcn_s_sleep(4);
          sweep<4>(rs, vo, qg);
        }
        FENCE();
        const f32x2 qs = (qq[0] + qq[1]) + (qq[2] + qq[3]);
        ldu[L_QPK + lane] = cvtpk(qs.x, qs.y);
        FENCE();
        if (lane == 0) ctl[1] = 1u;
        SB();
      }
      if (n == 16) {
        while (ctl[1] == 0u) __builtin_amdgcn_s_sleep(1);
        FENCE();
        qv = *(const u32x4*)&lds[L_QPK + 4 * c];
        tag0 = __builtin_amdgcn_readfirstlane(ctl[4]); tag1 = __builtin_amdgcn_readfirstlane(ctl[5]);
      }
      if constexpr (n >= 16 && n < 20) { SCORE(3 * (n - 16)); SCORE(3 * (n - 16) + 1); SCORE(3 * (n - 16) + 2); }
      if constexpr (n >= 20) { SCORE(12 + 2 * (n - 20)); SCORE(13 + 2 * (n - 20)); }
    };
    steps<1>(kstep, std::make_integer_sequence<int, 21>{});
    sc = trd<X1>(l4[0], l4[1], (c & 1) != 0);
  }
  const float s2 = bfr(bfr(sc) * 0.08838834764831845f) * 1.4426950408889634f;
  unsigned ppk;
  {
    const float mw = wave_max(s2);
    const float p = __builtin_amdgcn_exp2f(s2 - mw);
    ppk = cvtpk(p, dppf<X8>(p));
    const float lw = wave_sum(p);
    unsigned old = 0u;
    if (lane == 0) {
      f32x2 t = {mw, lw}; *(f32x2*)&lds[L_WML + 2 * w] = t;
      FENCE();
      old = __hip_atomic_fetch_add((LAS unsigned*)&ctl[2], 1u, __ATOMIC_RELAXED, __HIP_MEMORY_SCOPE_WORKGROUP);
    }
    if (__builtin_amdgcn_readfirstlane(old) == 15u) {
      FENCE();
      const f32x2 ml = *(const f32x2*)&lds[L_WML + 2 * c];
      const float Mwg = row_max(ml.x);
      const float Lwg = row_sum(ml.y * __builtin_amdgcn_exp2f(ml.x - Mwg));
      if (lane == 0) gst(rs, WS_ML + ((b * NH + head) * 4 + s) * 16, __float_as_uint(Mwg), __float_as_uint(Lwg), tag0, tag1);
    }
  }
  SB();
  REID();
  const int mloff = w == 0 ? WS_ML + ((b * NH + head) * 4 + (lane & 3)) * 16 : OOB;
  u32x4 mls;
  float wgt = 0.f;
  unsigned short xr;
  {
    float a[8];
#pragma unroll
    for (int i = 0; i < 8; ++i) a[i] = 0.f;
#define PV(u, L) do { const u32x4 v0 = vr[2 * (u)], v1 = vr[2 * (u) + 1]; const unsigned pp = dppu<BC + (L)>(ppk); \
      _Pragma("unroll") for (int j = 0; j < 4; ++j) { a[2 * j] = dot2(plo(v0[j], v1[j]), pp, a[2 * j]); a[2 * j + 1] = dot2(phi(v0[j], v1[j]), pp, a[2 * j + 1]); } \
      _Pragma("unroll") for (int i = 0; i < 8; ++i) PIN(a[i]); SB(); } while (0)
    auto vstep = [&](auto N_) __attribute__((always_inline)) {
      constexpr int n = decltype(N_)::value;
      PACE(n);
      vr[n - 16] = LDV(n - 16);
      if (n == 27) mls = gld(rs, mloff, 0);
      if constexpr (n == 23 || n == 24) {
        const int tt = tid - 640, Q = tt / 10, ch = tt - Q * 10;
        const rsrc_t rt = mk_rsrc(wd + (size_t)fcb * (64 * 1280) + frb * 160, (roleG && w >= 10 && w < 15) ? 63 * 1280 + 160 : 0);
        tw[n - 23] = bld<NTP>(rt, (2 * Q) * 1280 + ch * 16, (n - 23) * 1280);
      }
      SB();
      if (n == 28) {
        const int tt = tid - 640, Q = tt / 10, ch = tt - Q * 10;
        if (w >= 10 && w < 15) stash(ldu + L_PD + (8 * ch) * SD + Q, SD, tw[0], tw[1]);
        SB();
      }
      if (n == 22) PV(0, 0);
      if (n == 23) PV(1, 4);
      if (n == 25) PV(2, 2);
      if (n == 27) PV(3, 6);
      if (n == 29) PV(4, 1);
      if (n == 30 && w == 0) {
        for (unsigned spins = 0; !__all(tagok(mls, tag0, tag1)) && spins < SPIN_CAP; ++spins) {
          FENCE();
          mls = gld(rs, mloff, 0);
        }
        FENCE();
        const float ms = __uint_as_float(mls.x), ls = __uint_as_float(mls.z);
        float M = fmaxf(ms, dppf<X1>(ms)); M = fmaxf(M, dppf<X2>(M));
        float L = ls * __builtin_amdgcn_exp2f(ms - M);
        L += dppf<X1>(L); L += dppf<X2>(L);
        wgt = __builtin_amdgcn_exp2f(lds[L_WML + 2 * c] - M) / L;
        PIN(wgt);
        SB();
      }
    };
    steps<22>(vstep, std::make_integer_sequence<int, 10>{});
    PV(5, 5);
    PV(6, 3);
    WAIT_UNTIL(tstart + slip + PACE_OFF + (((30 * 16 + XTRA(31) + WO_LATE) * PU) >> 8));
    REID();
    {
      const rsrc_t rt = mk_rsrc(wo + (size_t)head * (128 * 1280) + sub * 160, (w >= 1 && w < 6) ? 127 * 1280 + 160 : 0);
#pragma unroll
      for (int e = 0; e < 2; ++e) {
        const int tt = (2 * (w - 1) + e) * 64 + lane, Q = tt / 10, ch = tt - Q * 10;
        tw[2 * e] = bld<NTP>(rt, (2 * Q) * 1280 + ch * 16, 0);
        tw[2 * e + 1] = bld<NTP>(rt, (2 * Q) * 1280 + ch * 16, 1280);
      }
    }
    xr = *(const GAS unsigned short*)(x_ + (lane >> 5) * (D * 2) + frb * 160 + ((w + 5) % 10) * 16 + (lane & 7) * 2);
    SB();
    PV(7, 7);
    float s4[4];
#pragma unroll
    for (int i = 0; i < 4; ++i) s4[i] = tr32(a[i], a[i + 4]);
    f32x2 u = {tr16(s4[0], s4[2]), tr16(s4[1], s4[3])};
    *(f32x2*)&lds[L_PART + w * 128 + 8 * c + 2 * r] = u;
  }
  wg_barrier();
  if (w == 0) {
    f32x2 pr[16];
#pragma unroll
    for (int ww = 0; ww < 16; ++ww) pr[ww] = *(const f32x2*)&lds[L_PART + ww * 128 + 2 * lane];
    f32x2 o2 = {0.f, 0.f}, o3 = {0.f, 0.f};
#pragma unroll
    for (int ww = 0; ww < 16; ww += 2) { o2 += pr[ww] * rdl(wgt, ww); o3 += pr[ww + 1] * rdl(wgt, ww + 1); }
    o2 += o3;
    gst(rs, WS_OX + lid * 1024 + l16, __float_as_uint(o2.x), __float_as_uint(o2.y), tag0, tag1);
  }
#define PARKED(i) do { FENCE(); if (lane == 0) __hip_atomic_fetch_add((LAS unsigned*)&ctl[i], 1u, __ATOMIC_RELAXED, __HIP_MEMORY_SCOPE_WORKGROUP); } while (0)
#define WAIT_PARKED(i) do { while (ctl[i] < 5u) __builtin_amdgcn_s_sleep(1); FENCE(); } while (0)
  u32x4 fb[6], tg[4];
  if (w >= 8) {
    const int bb = (w >> 2) & 1, Q = (w & 3) * 16 + c;
    int vo[1] = {WS_OX + (head * 8 + bb * 4 + r) * 1024 + Q * 16};
    f32x2 g[1];
    __builtin_amdgcn_s_sleep(D_OX);
    gather<1>(rs, vo, g, tag0, tag1);
    float o = tr32(g[0].x, g[0].y);
    o = tr16(o, o);
    if ((lane & 16) == 0) ((unsigned short*)(ldu + L_OA))[bb * 128 + 2 * Q + (lane >> 5)] = (unsigned short)cvtpk(o, 0.f);
  } else {
    if (w < 5) {
      const size_t go = (size_t)(80 * frb + 16 * w) * (FF * 2) + fcb * 128;
      const int nrec = roleG ? 15 * FF * 2 + 128 : 0;
      const rsrc_t rg = mk_rsrc(wg + go, nrec), ru = mk_rsrc(wu + go, nrec);
      const int vo = (2 * (lane >> 3)) * (FF * 2) + (lane & 7) * 16;
      tg[0] = bld<NTP>(rg, vo, 0); tg[1] = bld<NTP>(rg, vo, FF * 2);
      tg[2] = bld<NTP>(ru, vo, 0); tg[3] = bld<NTP>(ru, vo, FF * 2);
      SB();
    }
    if (w >= 1 && w < 6) {
#pragma unroll
      for (int e = 0; e < 2; ++e) {
        const int tt = (2 * (w - 1) + e) * 64 + lane, Q = tt / 10, ch = tt - Q * 10;
        stash(ldu + L_PO + (8 * ch) * SO + Q, SO, tw[2 * e], tw[2 * e + 1]);
      }
      PARKED(8);
    }
    if (w < 5) {
      WAIT_PARKED(8);
#pragma unroll
      for (int k = 0; k < 4; ++k) { fb[k] = *(const u32x4*)&ldu[L_PO + (16 * w + c) * SO + 16 * k + 4 * r]; PIN(fb[k]); }
    }
  }
  wg_barrier();
  const f32x4 zero4 = {0.f, 0.f, 0.f, 0.f};
  if (w < 5) {
    u32x4 fa[4];
#pragma unroll
    for (int k = 0; k < 4; ++k) fa[k] = *(const u32x4*)&ldu[L_OA + (lane & 1) * 64 + 16 * k + 4 * r];
    f32x4 d0 = mfma(fa[0], fb[0], zero4), d1 = mfma(fa[1], fb[1], zero4);
    d0 = mfma(fa[2], fb[2], d0); d1 = mfma(fa[3], fb[3], d1);
    d0 += d1;
    if (lane < 16)
      gst(rs, WS_Y + ((sub * 10 + 2 * w + (lane >> 3)) * 32 + head) * 128 + (lane & 7) * 16, __float_as_uint(d0[0]), __float_as_uint(d0[1]), tag0, tag1);
  }
  if (roleG) {
    const u32x4 ones4 = {0x3F803F80u, 0x3F803F80u, 0x3F803F80u, 0x3F803F80u};
    const int rec = fcb * 8 + frb;
    if (w < 5) {
      stash(ldu + L_PG + (8 * (lane & 7)) * SG + 8 * w + (lane >> 3), SG, tg[0], tg[1]);
      stash(ldu + L_PG + (64 + 8 * (lane & 7)) * SG + 8 * w + (lane >> 3), SG, tg[2], tg[3]);
      PARKED(9);
      if (w < 4) {
        WAIT_PARKED(9);
#pragma unroll
        for (int k = 0; k < 3; ++k) {
          fb[k] = *(const u32x4*)&ldu[L_PG + (16 * w + c) * SG + 16 * k + 4 * r];
          fb[3 + k] = *(const u32x4*)&ldu[L_PG + (64 + 16 * w + c) * SG + 16 * k + 4 * r];
          PIN(fb[k]); PIN(fb[3 + k]);
        }
        f32x4 cg = zero4, cu = zero4;
#pragma unroll
        for (int k = 0; k < 3; ++k) { cg = mfma(ones4, fb[k], cg); cu = mfma(ones4, fb[3 + k], cu); }
        if (lane < 16) gst(rs, WS_CS + rec * 1024 + w * 256 + l16, __float_as_uint(cg[0]), __float_as_uint(cu[0]), tag0, tag1);
      }
    }
    float hres = 0.f;
    const int j10 = w - 5;
    if (w >= 5 && w < 15) {
      const int vb = WS_Y + (frb * 10 + j10) * 4096 + lane * 16;
      int vo[4] = {vb, vb + 1024, vb + 2048, vb + 3072};
      f32x2 g[4];
      __builtin_amdgcn_s_sleep(D_Y);
      gather<4>(rs, vo, g, tag0, tag1);
      float at = sum8((g[0] + g[1]) + (g[2] + g[3]));
      at = bfr(at);
      const unsigned pk = cvtpk(__uint_as_float((unsigned)xr << 16) + at, 0.f);
      hres = bflo(pk);
      if ((lane & 24) == 0) ((unsigned short*)(ldu + L_HA))[(lane >> 5) * 96 + 8 * j10 + (lane & 7)] = (unsigned short)pk;
    }
    wg_barrier();
    if (w < 5) {
      u32x4 fa[3];
#pragma unroll
      for (int k = 0; k < 3; ++k) fa[k] = *(const u32x4*)&ldu[L_HA + (lane & 1) * 48 + 16 * k + 4 * r];
      if (w == 4) {
#pragma unroll
        for (int k = 0; k < 3; ++k) { fb[k] = c == 2 ? ones4 : fa[k]; fb[3 + k] = fb[k]; }
      }
      f32x4 d0 = mfma(fa[0], fb[0], zero4), d1 = mfma(fa[0], fb[3], zero4);
      d0 = mfma(fa[1], fb[1], d0); d1 = mfma(fa[1], fb[4], d1);
      d0 = mfma(fa[2], fb[2], d0); d1 = mfma(fa[2], fb[5], d1);
      const auto s0 = __builtin_amdgcn_permlane16_swap(__float_as_uint(d0[0]), __float_as_uint(d1[0]), false, false);
      const auto s1 = __builtin_amdgcn_permlane16_swap(__float_as_uint(d0[1]), __float_as_uint(d1[1]), false, false);
      if (lane < (w == 4 ? 3 : 32))
        gst(rs, WS_GP + rec * GP_REC + (w == 4 ? 2048 : w * 256 + r * 1024) + c * 16, s0[0], s1[0], tag0, tag1);
#pragma unroll
      for (int k = 0; k < 2; ++k) { fb[k] = *(const u32x4*)&ldu[L_PD + (16 * w + c) * SD + 16 * k + 4 * r]; PIN(fb[k]); }
    }
    if (w == 5) {
      int vo[1] = {(lane & 7) < 3 ? WS_GP + (fcb * 8 + (lane >> 3)) * GP_REC + 2048 + (lane & 7) * 16 : OOB};
      f32x2 g[1];
      __builtin_amdgcn_s_sleep(D_ST);
      gather<1>(rs, vo, g, tag0, tag1);
      const float st = sum8(g[0]);
      const float s2 = lane < 32 ? dppf<BC + 0>(st) : dppf<BC + 1>(st);
      const float mean = dppf<BC + 2>(st) * (1.f / D);
      const float var = fmaxf(fmaf(-mean, mean, s2 * (1.f / D)), 0.f);
      const f32x2 o = {bfr(mean), __builtin_amdgcn_rcpf(bfr(__builtin_amdgcn_sqrtf(bfr(bfr(var) + 1e-5f))))};
      if ((lane & 31) == 0) *(f32x2*)&lds[L_ST + (lane >> 5) * 2] = o;
      FENCE();
      if (lane == 0) ctl[11] = 1u;
    }
    if (w >= 8) {
      const int w8 = w - 8;
      const int vb = WS_GP + (fcb * 8 + (lane >> 3)) * GP_REC + (lane & 7) * 16;
      int vo[3] = {vb + w8 * 128, vb + 1024 + w8 * 128, WS_CS + (fcb * 8 + (lane >> 3)) * 1024 + w8 * 128 + (lane & 7) * 16};
      f32x2 g[3];
      __builtin_amdgcn_s_sleep(D_GP);
      gather<3>(rs, vo, g, tag0, tag1);
      const float gs = sum8(g[0]), us = sum8(g[1]), ct = sum8(g[2]);
      const auto sw = __builtin_amdgcn_permlane32_swap(__float_as_uint(ct), __float_as_uint(ct), false, false);
      const float cg = __uint_as_float(sw[0]), cu = __uint_as_float(sw[1]);
      while (ctl[11] == 0u) {}
      FENCE();
      const f32x2 st = *(const f32x2*)&lds[L_ST + (lane >> 5) * 2];
      const unsigned gu = cvtpk(fmaf(-st.x, cg, gs) * st.y, fmaf(-st.x, cu, us) * st.y);
      const unsigned hh = cvtpk(bflo(gu) * bfhi(gu), 0.f);
      if ((lane & 24) == 0) ((unsigned short*)(ldu + L_HH))[(lane >> 5) * 64 + 8 * w8 + (lane & 7)] = (unsigned short)hh;
    }
    wg_barrier();
    if (w < 5) {
      const u32x4 fa0 = *(const u32x4*)&ldu[L_HH + (lane & 1) * 32 + 4 * r], fa1 = *(const u32x4*)&ldu[L_HH + (lane & 1) * 32 + 16 + 4 * r];
      const f32x4 d0 = mfma(fa0, fb[0], zero4) + mfma(fa1, fb[1], zero4);
      if (lane < 16)
        gst(rs, WS_OP + ((frb * 10 + 2 * w + (lane >> 3)) * 32 + fcb) * 128 + (lane & 7) * 16, __float_as_uint(d0[0]), __float_as_uint(d0[1]), tag0, tag1);
    }
    if (j10 == fcb && fcb < 10) {
      const int vb = WS_OP + (frb * 10 + j10) * 4096 + lane * 16;
      int vo[4] = {vb, vb + 1024, vb + 2048, lane < 24 ? vb + 3072 : OOB};
      f32x2 g[4];
      __builtin_amdgcn_s_sleep(D_OP);
      gather<4>(rs, vo, g, tag0, tag1);
      float a = sum8((g[0] + g[1]) + (g[2] + g[3]));
      a = bfr(a);
      if ((lane & 24) == 0)
        *(GAS unsigned short*)(out + (lane >> 5) * (D * 2) + frb * 160 + j10 * 16 + (lane & 7) * 2) = (unsigned short)cvtpk(hres + a, 0.f);
      if (bid == 0 && lane == 0) {
        const u32x2 tg2 = {tag0, tag1};
        __builtin_amdgcn_raw_buffer_store_b64(tg2, rs, WS_EPOCH, 0, SC1);
      }
    }
  }
}

extern "C" void kernel_launch(void* const* d_in, const int* in_sizes, int n_in, void* d_out, int out_size, void* d_ws,
                              size_t ws_size, hipStream_t stream) {
  (void)in_sizes; (void)n_in; (void)out_size; (void)ws_size;
  fused_decode<<<NWG, NT, 0, stream>>>((const GAS char*)d_in[0], (const GAS char*)d_in[1], (const GAS char*)d_in[2],
                                       (const GAS char*)d_in[3], (const GAS char*)d_in[4], (const GAS char*)d_in[6],
                                       (const GAS char*)d_in[7], (const GAS char*)d_in[8], (GAS char*)d_out, (char*)d_ws);
}
```
